```python
import torch

def _softmax(x, axis=-1):
    return torch.softmax(x, dim=axis)
def _gelu(x):
    return torch.nn.functional.gelu(x, approximate="tanh")
def _silu(x):
    return torch.nn.functional.silu(x)
def _sigmoid(x):
    return torch.sigmoid(x)
def _layernorm(x, axis=-1, eps=1e-5):
    m = x.mean(dim=axis, keepdim=True)
    v = x.var(dim=axis, keepdim=True, unbiased=False)
    return (x - m) / torch.sqrt(v + eps)
def _scatter_add(tgt, idx, src, axis=0):
    return tgt.clone().index_add_(axis, idx, src)
def _segment_sum(src, seg, n):
    out = torch.zeros((n,) + src.shape[1:], dtype=src.dtype)
    return out.index_add_(0, seg, src)
def _pad(x, pw):
    flat = tuple(v for pair in reversed(pw) for v in pair)
    return torch.nn.functional.pad(x, flat)

kernel_name = 'concat_heads__sliding_block_cached__gqa_block_cached__decode'

def setup_inputs():

    _w = (lambda a: (lambda t: t.to(torch.bfloat16) if t.is_floating_point() else t)(torch.as_tensor(a)))
    def _A_setup():
        B, Sq, D = 8, 1, 512
        Hq, Hkv, Sk, Dh = 32, 8, 4096, 128
        assert Hq % Hkv == 0, (Hq, Hkv)
        return {
            "x": _w((lambda *s: torch.randn(*s, dtype=torch.bfloat16) * 0.8)(B, Sq, D)),
            "k_cache": _w((lambda *s: torch.randn(*s, dtype=torch.bfloat16) * 1.6)(B, Hkv, Sk, Dh)),
            "v_cache": _w((lambda *s: torch.randn(*s, dtype=torch.bfloat16) * 0.2)(B, Hkv, Sk, Dh)),
            "wq": _w((lambda *s: torch.randn(*s, dtype=torch.bfloat16) * 1.6)(D, Hq * Dh) * (D ** -0.5)),
            "wo": _w((lambda *s: torch.randn(*s, dtype=torch.bfloat16) * 0.2)(Hq * Dh, D) * ((Hq * Dh) ** -0.5)),
        }
    _A_in = _A_setup()

    def _B_setup():
        B, Sq, D = 8, 1, 512
        Hq, Hkv, Sk, Dh = 32, 8, 4096, 128
        assert Hq % Hkv == 0, (Hq, Hkv)
        return {
            "x": _w((lambda *s: torch.randn(*s, dtype=torch.bfloat16) * 0.8)(B, Sq, D)),
            "k_cache": _w((lambda *s: torch.randn(*s, dtype=torch.bfloat16) * 1.6)(B, Hkv, Sk, Dh)),
            "v_cache": _w((lambda *s: torch.randn(*s, dtype=torch.bfloat16) * 0.2)(B, Hkv, Sk, Dh)),
            "wq": _w((lambda *s: torch.randn(*s, dtype=torch.bfloat16) * 1.6)(D, Hq * Dh) * (D ** -0.5)),
            "wo": _w((lambda *s: torch.randn(*s, dtype=torch.bfloat16) * 0.2)(Hq * Dh, D) * ((Hq * Dh) ** -0.5)),
        }
    _B_in = _B_setup()

    return {
        "A_x": _A_in["x"],
        "A_k_cache": _A_in["k_cache"],
        "A_v_cache": _A_in["v_cache"],
        "A_wq": _A_in["wq"],
        "A_wo": _A_in["wo"],
        "B_x": _B_in["x"],
        "B_k_cache": _B_in["k_cache"],
        "B_v_cache": _B_in["v_cache"],
        "B_wq": _B_in["wq"],
        "B_wo": _B_in["wo"],
    }

def reference(A_x, A_k_cache, A_v_cache, A_wq, A_wo, B_x, B_k_cache, B_v_cache, B_wq, B_wo):
    def _A_ref(x, k_cache, v_cache, wq, wo):
        B, Sq, D = x.shape
        Hq, Dh = 32, 128
        Hkv, Sk = k_cache.shape[1], k_cache.shape[2]
        W = min(4096, max(256, Sk // 4))
        rep = Hq // Hkv
        q = (lambda x, *d: x.permute(*d))((x @ wq).reshape(B, Sq, Hq, Dh), 0, 2, 1, 3)
        k = (lambda x, n, axis: torch.repeat_interleave(x, n, dim=axis))(k_cache[:, :, Sk - W:, :], rep, axis=1)
        v = (lambda x, n, axis: torch.repeat_interleave(x, n, dim=axis))(v_cache[:, :, Sk - W:, :], rep, axis=1)
        scores = torch.einsum("bhqd,bhkd->bhqk", q, k) * (Dh ** -0.5)
        p = _softmax(scores, axis=-1)
        o = torch.einsum("bhqk,bhkd->bhqd", p, v)
        return (lambda x, *d: x.permute(*d))(o, 0, 2, 1, 3).reshape(B, Sq, Hq * Dh) @ wo

    def _B_ref(x, k_cache, v_cache, wq, wo):
        B, Sq, D = x.shape
        Hq, Dh = 32, 128
        Hkv = k_cache.shape[1]
        rep = Hq // Hkv
        q = (lambda x, *d: x.permute(*d))((x @ wq).reshape(B, Sq, Hq, Dh), 0, 2, 1, 3)
        k = (lambda x, n, axis: torch.repeat_interleave(x, n, dim=axis))(k_cache, rep, axis=1)
        v = (lambda x, n, axis: torch.repeat_interleave(x, n, dim=axis))(v_cache, rep, axis=1)
        scores = torch.einsum("bhqd,bhkd->bhqk", q, k) * (Dh ** -0.5)
        p = _softmax(scores, axis=-1)
        o = torch.einsum("bhqk,bhkd->bhqd", p, v)
        return (lambda x, *d: x.permute(*d))(o, 0, 2, 1, 3).reshape(B, Sq, Hq * Dh) @ wo

    a = _A_ref(A_x, A_k_cache, A_v_cache, A_wq, A_wo)
    b = _B_ref(B_x, B_k_cache, B_v_cache, B_wq, B_wo)
    return torch.concatenate([a, b], axis=-1)
```

```cpp
#include <hip/hip_runtime.h>
#include <stdint.h>
#pragma clang diagnostic ignored "-Wint-to-pointer-cast"

#define T_KB 459
#define T_START 800
constexpr int GATHER_W = 2;
constexpr int W_EARLY = 4;
constexpr int T0_MAX = 150;
constexpr int WO_AT = 80;
constexpr int D_POLL1 = 8;
constexpr int D_POLL2 = 20;
#define MAP5(a, b, c, d, e) ((u64)(a) | (u64)(b) << 5 | (u64)(c) << 10 | (u64)(d) << 15 | (u64)(e) << 20)
#define MAP_B (MAP5(12, 13, 19, 10, 11) | MAP5(15, 18, 14, 17, 16) << 25)
#define MAP_A (MAP5(12, 13, 17, 11, 16) | MAP5(10, 14, 15, 0, 0) << 25)
#define XW 8098054u
#define XI 34u

typedef unsigned u32;
typedef unsigned long long u64;
typedef u32 u32x2 __attribute__((ext_vector_type(2)));
typedef u32 u32x4 __attribute__((ext_vector_type(4)));
typedef float f32x2 __attribute__((ext_vector_type(2)));
typedef float f32x4 __attribute__((ext_vector_type(4)));
typedef __bf16 bf16x2 __attribute__((ext_vector_type(2)));
typedef __bf16 bf16x8 __attribute__((ext_vector_type(8)));
typedef short s16x4 __attribute__((ext_vector_type(4)));
typedef short s16x8 __attribute__((ext_vector_type(8)));
#define LAS __attribute__((address_space(3)))
#define GAS __attribute__((address_space(1)))
typedef __amdgpu_buffer_rsrc_t rsrc_t;

constexpr u32 KV_BYTES = 8u * 8u * 4096u * 128u * 2u;
constexpr u32 W_BYTES = 512u * 4096u * 2u;
constexpr u32 X_BYTES = 8u * 512u * 2u;
constexpr u32 WS_EPOCH = 0;
constexpr u32 WS_T0 = 256;
constexpr u32 WS_Q = 4096;
constexpr u32 REC = 72u * 16u;
constexpr u32 WS_O = WS_Q + 2u * 8u * 1024u * 16u;
constexpr u32 WS_Y = WS_O + 2u * 32u * 8u * 4u * REC;
constexpr u32 WS_END = WS_Y + 2u * 8u * 16u * 32u * 16u * 16u;
constexpr u32 L_RING = 0;
constexpr u32 L_W = 131072;
constexpr u32 L_RED = L_W;
constexpr u32 L_QF = L_W + 8192;
constexpr u32 L_QIMG = L_QF + 64;
constexpr u32 L_FLAG = L_RING + 8192 + 4096;
constexpr u32 L_OIMG = L_RING + 8192 + 4608;
constexpr u32 L_END = L_W + 32768;
constexpr u32 PSTR = 528;
constexpr float SC_LOG2E = 0.08838834764831845f * 1.4426950408889634f;
constexpr u32 SPIN_MAX = 1u << 15;

struct Params {
  const void* x[2];
  const void* k[2];
  const void* v[2];
  const void* wq[2];
  const void* wo[2];
  void* out;
  void* ws;
};

__device__ __forceinline__ rsrc_t mk_rsrc(const void* p, u32 bytes) {
  return __builtin_amdgcn_make_buffer_rsrc((void*)p, 0, bytes, 0x00020000);
}
__device__ __forceinline__ void dma4(rsrc_t r, u32 m, u32 v0, u32 v1, u32 v2, u32 v3, u32 s0, u32 s1, u32 s2, u32 s3) {
  asm volatile("s_mov_b32 m0, %[m]\n\ts_nop 0\n\t"
               "buffer_load_dwordx4 %[v0], %[r], %[s0] offen offset:0 nt lds\n\t"
               "buffer_load_dwordx4 %[v1], %[r], %[s1] offen offset:1024 nt lds\n\t"
               "buffer_load_dwordx4 %[v2], %[r], %[s2] offen offset:2048 nt lds\n\t"
               "buffer_load_dwordx4 %[v3], %[r], %[s3] offen offset:3072 nt lds"
               :: [m] "s"(m), [r] "s"(r), [v0] "v"(v0), [v1] "v"(v1), [v2] "v"(v2), [v3] "v"(v3),
                  [s0] "s"(s0), [s1] "s"(s1), [s2] "s"(s2), [s3] "s"(s3) : "memory");
}
__device__ __forceinline__ void dma1(rsrc_t r, u32 m, u32 v, u32 s) {
  asm volatile("s_mov_b32 m0, %[m]\n\ts_nop 0\n\tbuffer_load_dwordx4 %[v], %[r], %[s] offen lds"
               :: [m] "s"(m), [r] "s"(r), [v] "v"(v), [s] "s"(s) : "memory");
}
__device__ __forceinline__ void poll4t(u32x4 (&d)[5], rsrc_t r, u32 va, u32 vb, u32 vc, u32 t1, u32 t2) {
  u64 sv, t[10];
  u32 cnt;
  asm volatile("s_mov_b64 %[sv], exec\n\t"
               "s_mov_b64 %[m0], -1\n\t"
               "s_mov_b64 %[m1], -1\n\t"
               "s_mov_b64 %[m2], -1\n\t"
               "s_mov_b64 %[m3], -1\n\t"
               "s_mov_b64 %[m4], 0xff\n\t"
               "s_mov_b32 %[cnt], %[lim]\n"
               "1:\n\t"
               "s_mov_b64 exec, %[m0]\n\tbuffer_load_dwordx4 v[200:203], %[va], %[r], 0 offen sc1\n\t"
               "s_mov_b64 exec, %[m1]\n\tbuffer_load_dwordx4 v[204:207], %[va], %[r], 0 offen offset:1024 sc1\n\t"
               "s_mov_b64 exec, %[m2]\n\tbuffer_load_dwordx4 v[208:211], %[vb], %[r], 0 offen sc1\n\t"
               "s_mov_b64 exec, %[m3]\n\tbuffer_load_dwordx4 v[212:215], %[vb], %[r], 0 offen offset:1024 sc1\n\t"
               "s_mov_b64 exec, %[m4]\n\tbuffer_load_dwordx4 v[216:219], %[vc], %[r], 0 offen sc1\n\t"
               "s_mov_b64 exec, %[sv]\n\ts_waitcnt vmcnt(0)\n\t"
               "v_cmp_ne_u32_e64 %[m0], %[t1], v201\n\tv_cmp_ne_u32_e64 %[x0], %[t2], v203\n\t"
               "v_cmp_ne_u32_e64 %[m1], %[t1], v205\n\tv_cmp_ne_u32_e64 %[x1], %[t2], v207\n\t"
               "v_cmp_ne_u32_e64 %[m2], %[t1], v209\n\tv_cmp_ne_u32_e64 %[x2], %[t2], v211\n\t"
               "v_cmp_ne_u32_e64 %[m3], %[t1], v213\n\tv_cmp_ne_u32_e64 %[x3], %[t2], v215\n\t"
               "v_cmp_ne_u32_e64 %[m4], %[t1], v217\n\tv_cmp_ne_u32_e64 %[x4], %[t2], v219\n\t"
               "s_or_b64 %[m0], %[m0], %[x0]\n\t"
               "s_or_b64 %[m1], %[m1], %[x1]\n\t"
               "s_or_b64 %[m2], %[m2], %[x2]\n\t"
               "s_or_b64 %[m3], %[m3], %[x3]\n\t"
               "s_or_b64 %[m4], %[m4], %[x4]\n\t"
               "s_and_b64 %[m4], %[m4], 0xff\n\t"
               "s_or_b64 %[x0], %[m0], %[m1]\n\t"
               "s_or_b64 %[x0], %[x0], %[m2]\n\t"
               "s_or_b64 %[x0], %[x0], %[m3]\n\t"
               "s_or_b64 %[x0], %[x0], %[m4]\n\t"
               "s_cbranch_scc0 2f\n\t"
               "s_sub_u32 %[cnt], %[cnt], 1\n\ts_cbranch_scc0 1b\n"
               "2:"
               : "=&{v[200:203]}"(d[0]), "=&{v[204:207]}"(d[1]), "=&{v[208:211]}"(d[2]), "=&{v[212:215]}"(d[3]),
                 "=&{v[216:219]}"(d[4]), [sv] "=&s"(sv), [cnt] "=&s"(cnt), [m0] "=&s"(t[0]),
                 [m1] "=&s"(t[1]), [m2] "=&s"(t[2]), [m3] "=&s"(t[3]), [m4] "=&s"(t[4]),
                 [x0] "=&s"(t[5]), [x1] "=&s"(t[6]), [x2] "=&s"(t[7]), [x3] "=&s"(t[8]),
                 [x4] "=&s"(t[9])
               : [va] "v"(va), [vb] "v"(vb), [vc] "v"(vc), [r] "s"(r), [t1] "s"(t1), [t2] "s"(t2), [lim] "n"(SPIN_MAX)
               : "memory", "scc");
}
__device__ __forceinline__ void poll5(u32x4 (&d)[5], rsrc_t r, u32 va, u32 vb, u32 t1, u32 t2) {
  u64 sv, t[10];
  u32 cnt;
  asm volatile("s_mov_b64 %[sv], exec\n\t"
               "s_mov_b64 %[m0], -1\n\t"
               "s_mov_b64 %[m1], -1\n\t"
               "s_mov_b64 %[m2], -1\n\t"
               "s_mov_b64 %[m3], -1\n\t"
               "s_mov_b64 %[m4], 15\n\t"
               "s_mov_b32 %[cnt], %[lim]\n"
               "1:\n\t"
               "s_mov_b64 exec, %[m0]\n\tbuffer_load_dwordx4 v[200:203], %[va], %[r], 0 offen sc1\n\t"
               "s_mov_b64 exec, %[m1]\n\tbuffer_load_dwordx4 v[204:207], %[va], %[r], 0 offen offset:1152 sc1\n\t"
               "s_mov_b64 exec, %[m2]\n\tbuffer_load_dwordx4 v[208:211], %[va], %[r], 0 offen offset:2304 sc1\n\t"
               "s_mov_b64 exec, %[m3]\n\tbuffer_load_dwordx4 v[212:215], %[va], %[r], 0 offen offset:3456 sc1\n\t"
               "s_mov_b64 exec, %[m4]\n\tbuffer_load_dwordx4 v[216:219], %[vb], %[r], 0 offen sc1\n\t"
               "s_mov_b64 exec, %[sv]\n\ts_waitcnt vmcnt(0)\n\t"
               "v_cmp_ne_u32_e64 %[m0], %[t1], v201\n\tv_cmp_ne_u32_e64 %[x0], %[t2], v203\n\t"
               "v_cmp_ne_u32_e64 %[m1], %[t1], v205\n\tv_cmp_ne_u32_e64 %[x1], %[t2], v207\n\t"
               "v_cmp_ne_u32_e64 %[m2], %[t1], v209\n\tv_cmp_ne_u32_e64 %[x2], %[t2], v211\n\t"
               "v_cmp_ne_u32_e64 %[m3], %[t1], v213\n\tv_cmp_ne_u32_e64 %[x3], %[t2], v215\n\t"
               "v_cmp_ne_u32_e64 %[m4], %[t1], v217\n\tv_cmp_ne_u32_e64 %[x4], %[t2], v219\n\t"
               "s_or_b64 %[m0], %[m0], %[x0]\n\t"
               "s_or_b64 %[m1], %[m1], %[x1]\n\t"
               "s_or_b64 %[m2], %[m2], %[x2]\n\t"
               "s_or_b64 %[m3], %[m3], %[x3]\n\t"
               "s_or_b64 %[m4], %[m4], %[x4]\n\t"
               "s_and_b64 %[m4], %[m4], 15\n\t"
               "s_or_b64 %[x0], %[m0], %[m1]\n\t"
               "s_or_b64 %[x0], %[x0], %[m2]\n\t"
               "s_or_b64 %[x0], %[x0], %[m3]\n\t"
               "s_or_b64 %[x0], %[x0], %[m4]\n\t"
               "s_cbranch_scc0 2f\n\t"
               "s_sub_u32 %[cnt], %[cnt], 1\n\ts_cbranch_scc0 1b\n"
               "2:"
               : "=&{v[200:203]}"(d[0]), "=&{v[204:207]}"(d[1]), "=&{v[208:211]}"(d[2]), "=&{v[212:215]}"(d[3]),
                 "=&{v[216:219]}"(d[4]), [sv] "=&s"(sv), [cnt] "=&s"(cnt), [m0] "=&s"(t[0]),
                 [m1] "=&s"(t[1]), [m2] "=&s"(t[2]), [m3] "=&s"(t[3]), [m4] "=&s"(t[4]),
                 [x0] "=&s"(t[5]), [x1] "=&s"(t[6]), [x2] "=&s"(t[7]), [x3] "=&s"(t[8]),
                 [x4] "=&s"(t[9])
               : [va] "v"(va), [vb] "v"(vb), [r] "s"(r), [t1] "s"(t1), [t2] "s"(t2), [lim] "n"(SPIN_MAX)
               : "memory", "scc");
}
__device__ __forceinline__ void poll8(u32x4 (&d)[8], rsrc_t r, u32 va, u32 vb, u32 t1, u32 t2) {
  u64 sv, t[16];
  u32 cnt;
  asm volatile("s_mov_b64 %[sv], exec\n\t"
               "s_mov_b64 %[m0], -1\n\t"
               "s_mov_b64 %[m1], -1\n\t"
               "s_mov_b64 %[m2], -1\n\t"
               "s_mov_b64 %[m3], -1\n\t"
               "s_mov_b64 %[m4], -1\n\t"
               "s_mov_b64 %[m5], -1\n\t"
               "s_mov_b64 %[m6], -1\n\t"
               "s_mov_b64 %[m7], -1\n\t"
               "s_mov_b32 %[cnt], %[lim]\n"
               "1:\n\t"
               "s_mov_b64 exec, %[m0]\n\tbuffer_load_dwordx4 v[200:203], %[va], %[r], 0 offen sc1\n\t"
               "s_mov_b64 exec, %[m1]\n\tbuffer_load_dwordx4 v[204:207], %[va], %[r], 0 offen offset:1024 sc1\n\t"
               "s_mov_b64 exec, %[m2]\n\tbuffer_load_dwordx4 v[208:211], %[va], %[r], 0 offen offset:2048 sc1\n\t"
               "s_mov_b64 exec, %[m3]\n\tbuffer_load_dwordx4 v[212:215], %[va], %[r], 0 offen offset:3072 sc1\n\t"
               "s_mov_b64 exec, %[m4]\n\tbuffer_load_dwordx4 v[216:219], %[vb], %[r], 0 offen sc1\n\t"
               "s_mov_b64 exec, %[m5]\n\tbuffer_load_dwordx4 v[220:223], %[vb], %[r], 0 offen offset:1024 sc1\n\t"
               "s_mov_b64 exec, %[m6]\n\tbuffer_load_dwordx4 v[224:227], %[vb], %[r], 0 offen offset:2048 sc1\n\t"
               "s_mov_b64 exec, %[m7]\n\tbuffer_load_dwordx4 v[228:231], %[vb], %[r], 0 offen offset:3072 sc1\n\t"
               "s_mov_b64 exec, %[sv]\n\ts_waitcnt vmcnt(0)\n\t"
               "v_cmp_ne_u32_e64 %[m0], %[t1], v201\n\tv_cmp_ne_u32_e64 %[x0], %[t2], v203\n\t"
               "v_cmp_ne_u32_e64 %[m1], %[t1], v205\n\tv_cmp_ne_u32_e64 %[x1], %[t2], v207\n\t"
               "v_cmp_ne_u32_e64 %[m2], %[t1], v209\n\tv_cmp_ne_u32_e64 %[x2], %[t2], v211\n\t"
               "v_cmp_ne_u32_e64 %[m3], %[t1], v213\n\tv_cmp_ne_u32_e64 %[x3], %[t2], v215\n\t"
               "v_cmp_ne_u32_e64 %[m4], %[t1], v217\n\tv_cmp_ne_u32_e64 %[x4], %[t2], v219\n\t"
               "v_cmp_ne_u32_e64 %[m5], %[t1], v221\n\tv_cmp_ne_u32_e64 %[x5], %[t2], v223\n\t"
               "v_cmp_ne_u32_e64 %[m6], %[t1], v225\n\tv_cmp_ne_u32_e64 %[x6], %[t2], v227\n\t"
               "v_cmp_ne_u32_e64 %[m7], %[t1], v229\n\tv_cmp_ne_u32_e64 %[x7], %[t2], v231\n\t"
               "s_or_b64 %[m0], %[m0], %[x0]\n\t"
               "s_or_b64 %[m1], %[m1], %[x1]\n\t"
               "s_or_b64 %[m2], %[m2], %[x2]\n\t"
               "s_or_b64 %[m3], %[m3], %[x3]\n\t"
               "s_or_b64 %[m4], %[m4], %[x4]\n\t"
               "s_or_b64 %[m5], %[m5], %[x5]\n\t"
               "s_or_b64 %[m6], %[m6], %[x6]\n\t"
               "s_or_b64 %[m7], %[m7], %[x7]\n\t"
               "s_or_b64 %[x0], %[m0], %[m1]\n\t"
               "s_or_b64 %[x0], %[x0], %[m2]\n\t"
               "s_or_b64 %[x0], %[x0], %[m3]\n\t"
               "s_or_b64 %[x0], %[x0], %[m4]\n\t"
               "s_or_b64 %[x0], %[x0], %[m5]\n\t"
               "s_or_b64 %[x0], %[x0], %[m6]\n\t"
               "s_or_b64 %[x0], %[x0], %[m7]\n\t"
               "s_cbranch_scc0 2f\n\t"
               "s_sub_u32 %[cnt], %[cnt], 1\n\ts_cbranch_scc0 1b\n"
               "2:"
               : "=&{v[200:203]}"(d[0]), "=&{v[204:207]}"(d[1]), "=&{v[208:211]}"(d[2]), "=&{v[212:215]}"(d[3]),
                 "=&{v[216:219]}"(d[4]), "=&{v[220:223]}"(d[5]), "=&{v[224:227]}"(d[6]), "=&{v[228:231]}"(d[7]),
                 [sv] "=&s"(sv), [cnt] "=&s"(cnt), [m0] "=&s"(t[0]), [m1] "=&s"(t[1]),
                 [m2] "=&s"(t[2]), [m3] "=&s"(t[3]), [m4] "=&s"(t[4]), [m5] "=&s"(t[5]),
                 [m6] "=&s"(t[6]), [m7] "=&s"(t[7]), [x0] "=&s"(t[8]), [x1] "=&s"(t[9]),
                 [x2] "=&s"(t[10]), [x3] "=&s"(t[11]), [x4] "=&s"(t[12]), [x5] "=&s"(t[13]),
                 [x6] "=&s"(t[14]), [x7] "=&s"(t[15])
               : [va] "v"(va), [vb] "v"(vb), [r] "s"(r), [t1] "s"(t1), [t2] "s"(t2), [lim] "n"(SPIN_MAX)
               : "memory", "scc");
}
__device__ __forceinline__ void pin(u32& v) { asm volatile("" : "+v"(v)); }
__device__ __forceinline__ float lane_f(u32 v, int l) { return __uint_as_float(__builtin_amdgcn_readlane(v, l)); }
__device__ __forceinline__ void st16_sc1(u32x4 d, rsrc_t r, u32 v) {
  __builtin_amdgcn_raw_buffer_store_b128(d, r, v, 0, 16);
}
template <int N> __device__ __forceinline__ void wait_vm() { asm volatile("s_waitcnt vmcnt(%0)" :: "n"(N) : "memory"); }
__device__ __forceinline__ void wait_lgkm() { asm volatile("s_waitcnt lgkmcnt(0)" ::: "memory"); }
__device__ __forceinline__ void bar() { asm volatile("s_waitcnt lgkmcnt(0)\n\ts_barrier" ::: "memory"); }

__device__ __forceinline__ u32x4 lds_r128(u32 a) { return *(const LAS u32x4*)a; }
__device__ __forceinline__ f32x4 lds_rf4(u32 a) { return *(const LAS f32x4*)a; }
__device__ __forceinline__ f32x2 lds_rf2(u32 a) { return *(const LAS f32x2*)a; }
__device__ __forceinline__ s16x4 lds_tr(u32 a) { return __builtin_amdgcn_ds_read_tr16_b64_v4i16((LAS s16x4*)a); }
__device__ __forceinline__ bf16x8 cat_tr(s16x4 a, s16x4 b) {
  return __builtin_bit_cast(bf16x8, __builtin_shufflevector(a, b, 0, 1, 2, 3, 4, 5, 6, 7));
}
__device__ __forceinline__ u32 pk_bf16(float lo, float hi) {
  f32x2 f = {lo, hi};
  return __builtin_bit_cast(u32, __builtin_convertvector(f, bf16x2));
}
__device__ __forceinline__ f32x4 mfma(bf16x8 a, bf16x8 b, f32x4 c) { return __builtin_amdgcn_mfma_f32_16x16x32_bf16(a, b, c, 0, 0, 0); }
template <int CTRL> __device__ __forceinline__ float dpp(float x) {
  return __uint_as_float(__builtin_amdgcn_update_dpp(0u, __float_as_uint(x), CTRL, 0xf, 0xf, true));
}
__device__ __forceinline__ float vmax(float a, float b) {
  float r; asm("v_max_f32 %0, %1, %2" : "=v"(r) : "v"(a), "v"(b)); return r;
}
__device__ __forceinline__ float quad_max(float x) {
  asm("s_nop 1\n\tv_max_f32_dpp %0, %0, %0 quad_perm:[1,0,3,2] row_mask:0xf bank_mask:0xf\n\t"
      "s_nop 1\n\tv_max_f32_dpp %0, %0, %0 quad_perm:[2,3,0,1] row_mask:0xf bank_mask:0xf" : "+v"(x));
  return x;
}
__device__ __forceinline__ float fold32(float a, float b) {
  auto s = __builtin_amdgcn_permlane32_swap(__float_as_uint(a), __float_as_uint(b), false, false);
  return __uint_as_float(s[0]) + __uint_as_float(s[1]);
}
__device__ __forceinline__ float xrow_max(float x) {
  auto s = __builtin_amdgcn_permlane16_swap(__float_as_uint(x), __float_as_uint(x), false, false);
  x = fmaxf(__uint_as_float(s[0]), __uint_as_float(s[1]));
  auto t = __builtin_amdgcn_permlane32_swap(__float_as_uint(x), __float_as_uint(x), false, false);
  return fmaxf(__uint_as_float(t[0]), __uint_as_float(t[1]));
}
__device__ __forceinline__ float xrow_sum(float x) {
  auto s = __builtin_amdgcn_permlane16_swap(__float_as_uint(x), __float_as_uint(x), false, false);
  x = __uint_as_float(s[0]) + __uint_as_float(s[1]);
  auto t = __builtin_amdgcn_permlane32_swap(__float_as_uint(x), __float_as_uint(x), false, false);
  return __uint_as_float(t[0]) + __uint_as_float(t[1]);
}

struct Soft { float m, l; f32x4 acc[8]; };

__device__ __forceinline__ void load_k(u32x4 (&kf)[2][4], const u32 (&ka)[4]) {
#pragma unroll
  for (int u = 0; u < 2; ++u)
#pragma unroll
    for (int kk = 0; kk < 4; ++kk) kf[u][kk] = lds_r128(ka[kk] + u * 4096);
  wait_lgkm();
}
__device__ __forceinline__ void load_v(s16x4 (&vf)[8][2], const u32 (&va)[8]) {
#pragma unroll
  for (int dt = 0; dt < 8; ++dt) {
    vf[dt][0] = lds_tr(va[dt]);
    vf[dt][1] = lds_tr(va[dt] + 4096);
  }
  wait_lgkm();
}
template <bool FIRST>
__device__ __forceinline__ bf16x8 scores(Soft& st, const u32x4 (&kf)[2][4], const bf16x8 (&qf)[4]) {
  f32x4 s0 = {0.f, 0.f, 0.f, 0.f}, s1 = {0.f, 0.f, 0.f, 0.f};
#pragma unroll
  for (int kk = 0; kk < 4; ++kk) {
    s0 = mfma(__builtin_bit_cast(bf16x8, kf[0][kk]), qf[kk], s0);
    s1 = mfma(__builtin_bit_cast(bf16x8, kf[1][kk]), qf[kk], s1);
  }
  float mx = fmaxf(fmaxf(fmaxf(s0[0], s0[1]), fmaxf(s0[2], s0[3])), fmaxf(fmaxf(s1[0], s1[1]), fmaxf(s1[2], s1[3])));
  mx = xrow_max(mx);
  const float mn = FIRST ? mx : fmaxf(st.m, mx);
  const float mc = mn * SC_LOG2E;
  float pr[8];
#pragma unroll
  for (int j = 0; j < 4; ++j) {
    pr[j] = __builtin_amdgcn_exp2f(__builtin_fmaf(s0[j], SC_LOG2E, -mc));
    pr[4 + j] = __builtin_amdgcn_exp2f(__builtin_fmaf(s1[j], SC_LOG2E, -mc));
  }
  const float ps = ((pr[0] + pr[1]) + (pr[2] + pr[3])) + ((pr[4] + pr[5]) + (pr[6] + pr[7]));
  u32x4 pk = {pk_bf16(pr[0], pr[1]), pk_bf16(pr[2], pr[3]), pk_bf16(pr[4], pr[5]), pk_bf16(pr[6], pr[7])};
  if (FIRST) {
    st.l = ps;
  } else {
    const float alpha = __builtin_amdgcn_exp2f(st.m * SC_LOG2E - mc);
    st.l = __builtin_fmaf(st.l, alpha, ps);
#pragma unroll
    for (int dt = 0; dt < 8; ++dt) st.acc[dt] *= alpha;
  }
  st.m = mn;
  return __builtin_bit_cast(bf16x8, pk);
}
template <bool FIRST>
__device__ __forceinline__ void accumulate(Soft& st, const s16x4 (&vf)[8][2], bf16x8 pf) {
#pragma unroll
  for (int dt = 0; dt < 8; ++dt) {
    const f32x4 z = {0.f, 0.f, 0.f, 0.f};
    st.acc[dt] = mfma(cat_tr(vf[dt][0], vf[dt][1]), pf, FIRST ? z : st.acc[dt]);
  }
}
template <int WAIT_K, int WAIT_V, bool FIRST, class FK, class FV>
__device__ __forceinline__ void attn_block(Soft& st, const bf16x8 (&qf)[4], const u32 (&ka)[4], const u32 (&va)[8], FK issue_k, FV issue_v) {
  wait_vm<WAIT_K>();
  u32x4 kf[2][4];
  load_k(kf, ka);
  issue_k();
  const bf16x8 pf = scores<FIRST>(st, kf, qf);
  wait_vm<WAIT_V>();
  s16x4 vf[8][2];
  load_v(vf, va);
  issue_v();
  accumulate<FIRST>(st, vf, pf);
}

__global__ void __launch_bounds__(512, 2) fused_attn_kernel(Params p) {
  __shared__ __attribute__((aligned(16))) char smem[L_END];
  const u32 lds0 = (u32)(uintptr_t)(LAS char*)smem;
  const int tid = threadIdx.x, lane = tid & 63;
  const int w = __builtin_amdgcn_readfirstlane(tid >> 6);
  const int g = lane >> 4, n = lane & 15;
  const int wg = blockIdx.x, xc = wg & 7, slot = wg >> 3;
  const u32 t_wave = (u32)__builtin_amdgcn_s_memrealtime();
  const void *px0 = p.x[0], *px1 = p.x[1], *pk0 = p.k[0], *pk1 = p.k[1], *pv0 = p.v[0], *pv1 = p.v[1];
  const void *pq0 = p.wq[0], *pq1 = p.wq[1], *po0 = p.wo[0], *po1 = p.wo[1];
  void *pout = p.out, *pws = p.ws;
  asm volatile("" : "+s"(px0), "+s"(px1), "+s"(pk0), "+s"(pk1), "+s"(pv0), "+s"(pv1));
  asm volatile("" : "+s"(pq0), "+s"(pq1), "+s"(po0), "+s"(po1), "+s"(pout), "+s"(pws));

  const u32 kslot = lds0 + L_RING + w * 16384, vslot = kslot + 8192;
  const int br2 = slot >> 4;

  const int tb = xc, th = slot >> 2, tj = slot & 3;
  const u32 pair_off = (u32)(tb * 8 + th) * (4096u * 256u);
  constexpr u64 mapb = MAP_B, mapa = MAP_A;
  constexpr u32 xw = XW, xi = XI;
  auto put = [](u64 map, int k, u32 bit) { return (bit & 1) << ((u32)(map >> (5 * k)) & 31); };
  u32 wr = w, ir = 0;
  {
    const u32 id = tj | th << 2 | tb << 5;
#pragma unroll
    for (int k = 0; k < 8; ++k) {
      if ((id >> k) & 1) { wr ^= (xw >> (3 * k)) & 7; ir ^= (xi >> (2 * k)) & 3; }
    }
  }
  const u32 sA = pair_off + 3072u * 256u + put(mapa, 3, wr) + put(mapa, 4, wr >> 1) + put(mapa, 5, wr >> 2) + put(mapa, 6, tj) + put(mapa, 7, tj >> 1);
  const u32 sB = pair_off + put(mapb, 3, wr) + put(mapb, 4, wr >> 1) + put(mapb, 5, wr >> 2) + put(mapb, 8, tj) + put(mapb, 9, tj >> 1);
  auto blk = [&](u32 i) { return sB + put(mapb, 6, i ^ ir) + put(mapb, 7, (i ^ ir) >> 1); };
  u32 kv[4], vv[2];
#pragma unroll
  for (int t = 0; t < 4; ++t) kv[t] = g * 256 + 16 * (n ^ ((4 * t + g) & 15));
#pragma unroll
  for (int t = 0; t < 2; ++t) vv[t] = g * 256 + 16 * (n ^ (((4 * t + g) & 7) << 1));
  const rsrc_t rkA = mk_rsrc(pk0, KV_BYTES), rvA = mk_rsrc(pv0, KV_BYTES);
  const rsrc_t rkB = mk_rsrc(pk1, KV_BYTES), rvB = mk_rsrc(pv1, KV_BYTES);
  auto issue_kv = [&](rsrc_t r, u32 slotaddr, u32 s, bool isv, u64 map) {
    const u32 d1 = put(map, 0, 1) - 1024, d2 = put(map, 1, 1) - 2048, s2 = s + put(map, 2, 1);
    if (isv) {
      dma4(r, slotaddr, vv[0], vv[1], vv[0], vv[1], s, s + d1, s + d2, s + d1 + d2);
      dma4(r, slotaddr + 4096, vv[0], vv[1], vv[0], vv[1], s2, s2 + d1, s2 + d2, s2 + d1 + d2);
    } else {
      dma4(r, slotaddr, kv[0], kv[1], kv[2], kv[3], s, s + d1, s + d2, s + d1 + d2);
      dma4(r, slotaddr + 4096, kv[0], kv[1], kv[2], kv[3], s2, s2 + d1, s2 + d2, s2 + d1 + d2);
    }
  };
  const int cg = xc * 16 + (slot & 15);
  {
    const rsrc_t rx = mk_rsrc(br2 ? px1 : px0, X_BYTES), rw = mk_rsrc(br2 ? pq1 : pq0, W_BYTES);
    const u32 wv = (lane >> 2) * 8192 + 16 * ((lane & 3) ^ (((lane >> 5) & 1) << 1));
    const u32 ws0 = (u32)w * (64u * 8192u) + cg * 64;
    dma1(rx, lds0 + L_RED + w * 1024, (lane >> 3) * 1024 + (lane & 7) * 16, w * 128);
    dma4(rw, vslot, wv, wv, wv, wv, ws0, ws0 + (131072 - 1024), ws0 + 2 * (131072 - 1024), ws0 + 3 * (131072 - 1024));
  }
  u64 ep;
  asm volatile("s_load_dwordx2 %0, %1, 0x0" : "=s"(ep) : "s"(pws) : "memory");
  if (lane == 0) *(LAS u32*)(lds0 + L_FLAG + w * 4) = 0;
  if (tid < 2) *(LAS u32*)(lds0 + L_QF + tid * 4) = 0;
  bar();
  asm volatile("" : "+s"(ep));
  ep += 1;
  const u32 t1 = (u32)ep, t2 = (u32)ep ^ (u32)(ep >> 32);
  const rsrc_t rws = mk_rsrc(pws, WS_END);

  const int phase = (w + 7) & 7;
  constexpr u32 tkb = T_KB, tstart = T_START;
  const u32 t_off = (tstart + (u32)phase * tkb) >> 4;
  u32 t_base = t_wave + t_off;
  const int oh = xc * 4 + ((slot >> 2) & 3), ocg = slot & 3;
  auto issue_wo = [&](int r16) {
    const rsrc_t rwo = mk_rsrc(br2 ? po1 : po0, W_BYTES);
    const u32 v0 = g * 1024 + 16 * (n ^ ((g & 7) << 1)), v1 = g * 1024 + 16 * (n ^ (((4 + g) & 7) << 1));
    const u32 s = (u32)(oh * 128 + r16 * 16) * 1024u + ocg * 256;
    dma4(rwo, lds0 + L_W + r16 * 4096, v0, v1, v0, v1, s, s + 3072, s + 2 * 3072, s + 3 * 3072);
  };
  auto pace = [&](int kb, u32 extra = 0) {
    const u32 due = t_base + extra + (((u32)kb * tkb) >> 4);
#pragma nounroll
    for (u32 spin = 0; spin < SPIN_MAX; ++spin) {
      if ((int)((u32)__builtin_amdgcn_s_memrealtime() - due) >= 0) break;
      __builtin_amdgcn_s_sleep(1);
    }
  };
  auto gather_q = [&]() {
    u32x4 d[5];
    const u32 qoff = WS_Q + (tb * 1024 + th * 128 + lane) * 16;
    poll4t(d, rws, qoff, qoff + 8 * 1024 * 16, WS_T0 + (lane & 7) * 16, t1, t2);
    int late = 0;
#pragma unroll
    for (int i = 0; i < 8; ++i) late = max(late, (int)(t_wave - __builtin_amdgcn_readlane(d[4][0], i)));
    late = min(late, T0_MAX);
    if (lane == 0) *(LAS u32*)(lds0 + L_QF + 8) = t_wave - (u32)late;
#pragma unroll
    for (int i = 0; i < 4; ++i) {
      const int idx = (i & 1) * 64 + lane;
      u32x2 qv = {d[i][0], d[i][2]};
      *(LAS u32x2*)(lds0 + L_QIMG + ((i >> 1) * 4 + (idx >> 5)) * 272 + (idx & 31) * 8) = qv;
    }
    wait_lgkm();
    if (lane == 0) *(volatile LAS u32*)(lds0 + L_QF) = 1;
  };
  if (phase < W_EARLY) { pace(0); issue_kv(rkA, kslot, sA, false, mapa); wait_vm<8>(); }
  else wait_vm<0>();
  {
    f32x4 qa[2] = {{0.f, 0.f, 0.f, 0.f}, {0.f, 0.f, 0.f, 0.f}};
    const int q = (lane >> 2) & 3, pp = lane & 3;
    const u32x4 xf0 = lds_r128(lds0 + L_RED + w * 1024 + (n & 7) * 128 + g * 16);
    const u32x4 xf1 = lds_r128(lds0 + L_RED + w * 1024 + (n & 7) * 128 + 64 + g * 16);
    s16x4 wf[2][2][2];
#pragma unroll
    for (int kk = 0; kk < 2; ++kk)
#pragma unroll
      for (int ct = 0; ct < 2; ++ct)
#pragma unroll
        for (int u = 0; u < 2; ++u)
          wf[kk][ct][u] = lds_tr(vslot + (32 * kk + 8 * g + 4 * u + q) * 64 + 16 * ((2 * ct + (pp >> 1)) ^ ((g & 1) << 1)) + 8 * (pp & 1));
#pragma unroll
    for (int ct = 0; ct < 2; ++ct) {
      qa[ct] = mfma(cat_tr(wf[0][ct][0], wf[0][ct][1]), __builtin_bit_cast(bf16x8, xf0), qa[ct]);
      qa[ct] = mfma(cat_tr(wf[1][ct][0], wf[1][ct][1]), __builtin_bit_cast(bf16x8, xf1), qa[ct]);
    }
    const f32x4 qs = n < 8 ? qa[0] : qa[1];
    wait_lgkm();
    *(LAS f32x4*)(lds0 + L_RED + (w * 256 + (n & 7) * 32 + (n >> 3) * 16 + 4 * g) * 4) = qs;
    wait_lgkm();
    if (lane == 0) *(volatile LAS u32*)(lds0 + L_FLAG + w * 4) = 1;
  }
  if (w != 0) {
    if (phase >= W_EARLY) { pace(0); issue_kv(rkA, kslot, sA, false, mapa); }
  } else {
    for (u32 spin = 0; spin < SPIN_MAX * 8; ++spin) {
      const u32x4 f0 = *(volatile LAS u32x4*)(lds0 + L_FLAG), f1 = *(volatile LAS u32x4*)(lds0 + L_FLAG + 16);
      if ((f0[0] & f0[1] & f0[2] & f0[3] & f1[0] & f1[1] & f1[2] & f1[3]) == 1) break;
    }
    const int b = lane >> 3, c4 = lane & 7;
    f32x4 s = lds_rf4(lds0 + L_RED + (b * 32 + c4 * 4) * 4);
#pragma unroll
    for (int i = 1; i < 8; ++i) s += lds_rf4(lds0 + L_RED + (i * 256 + b * 32 + c4 * 4) * 4);
    u32x4 u = {pk_bf16(s[0], s[1]), t1, pk_bf16(s[2], s[3]), t2};
    st16_sc1(u, rws, WS_Q + ((br2 * 8 + b) * 1024 + cg * 8 + c4) * 16);
    if (wg < 8 && lane == 0) st16_sc1(u32x4{t_wave, t1, t_wave, t2}, rws, WS_T0 + wg * 16);
    pace(0); issue_kv(rkA, kslot, sA, false, mapa);
  }
  pace(8); issue_kv(rvA, vslot, sA, true, mapa);

  {
    u32 ka[4], va[8];
#pragma unroll
    for (int kk = 0; kk < 4; ++kk) ka[kk] = kslot + n * 256 + 16 * ((4 * kk + g) ^ n);
    {
      const int q = (lane >> 2) & 3, pp = lane & 3, r = 4 * g + q;
#pragma unroll
      for (int dt = 0; dt < 8; ++dt) va[dt] = vslot + r * 256 + 32 * (dt ^ (r & 7)) + 8 * pp;
    }
    Soft sa, sb;
    bf16x8 qg[4];
    {
      u32x4 kf[2][4];
      s16x4 vf[8][2];
      wait_vm<8>();
      load_k(kf, ka);
      if (w == GATHER_W) gather_q();
      pace(16); issue_kv(rkB, kslot, blk(0), false, mapb);
      wait_vm<8>();
      load_v(vf, va);
      pace(24); issue_kv(rvB, vslot, blk(0), true, mapb);
      for (u32 spin = 0; spin < SPIN_MAX * 8; ++spin) {
        if (*(volatile LAS u32*)(lds0 + L_QF) == 1) break;
        __builtin_amdgcn_s_sleep(1);
      }
      asm volatile("" ::: "memory");
      t_base = __builtin_amdgcn_readfirstlane(*(const LAS u32*)(lds0 + L_QF + 8)) + t_off;
      bf16x8 qf[4];
#pragma unroll
      for (int kk = 0; kk < 4; ++kk) {
        qf[kk] = __builtin_bit_cast(bf16x8, lds_r128(lds0 + L_QIMG + (n & 3) * 272 + (32 * kk + 8 * g) * 2));
        qg[kk] = __builtin_bit_cast(bf16x8, lds_r128(lds0 + L_QIMG + (4 + (n & 3)) * 272 + (32 * kk + 8 * g) * 2));
      }
      wait_lgkm();
      if (lane == 0) __hip_atomic_fetch_add((LAS u32*)(lds0 + L_QF + 4), 1u, __ATOMIC_RELAXED, __HIP_MEMORY_SCOPE_WORKGROUP);
      const bf16x8 pf = scores<true>(sa, kf, qf);
      accumulate<true>(sa, vf, pf);
    }
    attn_block<8, 8, true>(sb, qg, ka, va,
                           [&]() { pace(32); issue_kv(rkB, kslot, blk(1), false, mapb); },
                           [&]() { pace(40); issue_kv(rvB, vslot, blk(1), true, mapb); });
    attn_block<8, 8, false>(sb, qg, ka, va,
                            [&]() { pace(48); issue_kv(rkB, kslot, blk(2), false, mapb); },
                            [&]() { pace(56); issue_kv(rvB, vslot, blk(2), true, mapb); });
    attn_block<8, 8, false>(sb, qg, ka, va,
                            [&]() { pace(64); issue_kv(rkB, kslot, blk(3), false, mapb); },
                            [&]() { pace(72); issue_kv(rvB, vslot, blk(3), true, mapb); });
    auto park = [&](const Soft& st, int br) {
      const float l = xrow_sum(st.l);
      if (n < 4) {
#pragma unroll
        for (int dt = 0; dt < 8; ++dt) *(LAS f32x4*)(kslot + (br * 4 + n) * PSTR + (16 * dt + 4 * g) * 4) = st.acc[dt];
        if (g == 0) *(LAS f32x2*)(kslot + 8 * PSTR + w * 64 + (br * 4 + n) * 8) = f32x2{st.m * SC_LOG2E, l};
      }
    };
    const int hi = lane >> 5, l5 = lane & 31;
    u32 a_ml = lds0 + L_RING + (4 * hi + (lane & 3)) * (16384 + 64) + 8 * PSTR + w * 8;
    u32 a_ov = lds0 + L_RING + 4 * hi * 16384 + w * PSTR + l5 * 16;
    u32 a_pub = WS_O + (u32)((((w >> 2) * 32 + th * 4 + (w & 3)) * 8 + tb) * 4 + tj) * REC + lane * 16;
    const u32 rid = ((br2 * 32 + oh) * 8 + w) * 4;
    u32 a_d = WS_O + rid * REC + lane * 16, a_e = WS_O + (rid + (lane & 3)) * REC + 1024;
    u32 a_oimg, a_w, a_of, a_y, y_src;
    {
      const int dd = 4 * l5 + 2 * hi, r = dd & 31;
      a_oimg = lds0 + L_OIMG + w * 272 + ((dd & ~31) + ((r & 15) >> 2) * 8 + (r >> 4) * 4 + (r & 3)) * 2;
      const int q = (lane >> 2) & 3, pp = lane & 3, rr = 4 * g + q;
      a_w = lds0 + L_W + rr * 256 + 32 * (w ^ (rr & 7)) + 8 * pp;
      a_of = lds0 + L_OIMG + (n & 7) * 272 + 8 * g * 2;
      const u32 yid = ((br2 * 8 + (lane >> 3)) * 16 + ocg * 4 + (w >> 1)) * 32 + oh;
      a_y = WS_Y + (yid * 16 + (w & 1) * 8 + (lane & 7)) * 16;
      y_src = (((lane & 7) >> 1) * 16 + (lane & 1) * 8 + (lane >> 3)) * 4;
    }
    attn_block<8, 0, false>(sb, qg, ka, va, [&]() {
      park(sa, 0);
      pin(a_ml); pin(a_ov); pin(a_pub); pin(a_d); pin(a_e); pin(a_oimg); pin(a_w); pin(a_of); pin(a_y); pin(y_src);
    }, [&]() {});
    park(sb, 1);
    if (phase < 4) {
      if (phase <= 1) {
        for (u32 spin = 0; spin < SPIN_MAX * 8; ++spin)
          if (*(volatile LAS u32*)(lds0 + L_QF + 4) == 8u + phase) break;
        if (phase == 0 && lane == 0) *(volatile LAS u32*)(lds0 + L_QF + 4) = 9;
      }
      pace(0, ((u32)(WO_AT - phase) * tkb) >> 4);
      issue_wo(2 * phase);
      issue_wo(2 * phase + 1);
    }
    bar();
    {
      const f32x2 ml = lds_rf2(a_ml);
      f32x4 ov[4];
#pragma unroll
      for (int i = 0; i < 4; ++i) ov[i] = lds_rf4(a_ov + i * 16384);
      asm volatile("" : "+v"(ov[0]), "+v"(ov[1]), "+v"(ov[2]), "+v"(ov[3]));
      float M = quad_max(ml[0]);
      { auto sw = __builtin_amdgcn_permlane32_swap(__float_as_uint(M), __float_as_uint(M), false, false);
        M = vmax(__uint_as_float(sw[0]), __uint_as_float(sw[1])); }
      const float wt = __builtin_amdgcn_exp2f(ml[0] - M);
      float L = wt * ml[1];
      L += dpp<0xB1>(L); L += dpp<0x4E>(L);
      L = fold32(L, L);
      f32x4 o = dpp<0x00>(wt) * ov[0];
      o += dpp<0x55>(wt) * ov[1];
      o += dpp<0xAA>(wt) * ov[2];
      o += dpp<0xFF>(wt) * ov[3];
      const float o0 = fold32(o[0], o[2]), o1 = fold32(o[1], o[3]);
      u32x4 uo = {__float_as_uint(o0), t1, __float_as_uint(o1), t2};
      st16_sc1(uo, rws, a_pub);
      if (lane < 8) {
        u32x4 um = {__float_as_uint(M), t1, __float_as_uint(L), t2};
        st16_sc1(um, rws, a_pub + 1024);
      }
    }

    s16x4 wf[8];
    wait_vm<2>();
    bar();
#pragma unroll
    for (int c = 0; c < 8; ++c) wf[c] = lds_tr(a_w + c * 4096);
    {
      u32x4 d[5];
      __builtin_amdgcn_s_sleep(D_POLL1);
      poll5(d, rws, a_d, a_e, t1, t2);
      float Mj[4], Lj[4];
#pragma unroll
      for (int j = 0; j < 4; ++j) { Mj[j] = lane_f(d[4][0], j); Lj[j] = lane_f(d[4][2], j); }
      const float M = vmax(vmax(Mj[0], Mj[1]), vmax(Mj[2], Mj[3]));
      float L = 0.f, o0 = 0.f, o1 = 0.f;
#pragma unroll
      for (int j = 0; j < 4; ++j) {
        const float wt = __builtin_amdgcn_exp2f(Mj[j] - M);
        L = __builtin_fmaf(wt, Lj[j], L);
        o0 = __builtin_fmaf(wt, __uint_as_float(d[j][0]), o0);
        o1 = __builtin_fmaf(wt, __uint_as_float(d[j][2]), o1);
      }
      const float inv = __builtin_amdgcn_rcpf(L);
      *(LAS u32*)a_oimg = pk_bf16(o0 * inv, o1 * inv);
    }
    bar();
    f32x4 y = {0.f, 0.f, 0.f, 0.f};
#pragma unroll
    for (int kk = 0; kk < 4; ++kk)
      y = mfma(cat_tr(wf[2 * kk], wf[2 * kk + 1]), __builtin_bit_cast(bf16x8, lds_r128(a_of + kk * 64)), y);
    const float ys0 = n < 8 ? y[0] : y[2], ys1 = n < 8 ? y[1] : y[3];
    const u32 y0 = __builtin_amdgcn_ds_bpermute(y_src, __float_as_uint(ys0)), y1 = __builtin_amdgcn_ds_bpermute(y_src, __float_as_uint(ys1));
    u32x4 uy = {y0, t1, y1, t2};
    st16_sc1(uy, rws, a_y);
  }

  if (w == 0) {
    const int b = xc, c32 = slot & 15;
    const u32 base = WS_Y + (u32)((br2 * 8 + b) * 16 + c32) * (32u * 256u) + lane * 16;
    u32x4 d[8];
    __builtin_amdgcn_s_sleep(D_POLL2);
    poll8(d, rws, base, base + 4096, t1, t2);
    float y0 = 0.f, y1 = 0.f;
#pragma unroll
    for (int i = 0; i < 8; ++i) { y0 += __uint_as_float(d[i][0]); y1 += __uint_as_float(d[i][2]); }
    y0 = xrow_sum(y0); y1 = xrow_sum(y1);
    if (lane < 16) {
      GAS u32* po = (GAS u32*)pout + (b * 1024 + br2 * 512 + c32 * 32) / 2 + lane;
      *po = pk_bf16(y0, y1);
    }
    if (wg == 0 && lane == 0) __hip_atomic_store((GAS u64*)((GAS char*)pws + WS_EPOCH), ep, __ATOMIC_RELAXED, __HIP_MEMORY_SCOPE_AGENT);
  }
}

extern "C" void kernel_launch(void* const* d_in, const int* in_sizes, int n_in, void* d_out, int out_size,
                              void* d_ws, size_t ws_size, hipStream_t stream) {
  Params p;
  p.x[0] = d_in[0]; p.k[0] = d_in[1]; p.v[0] = d_in[2]; p.wq[0] = d_in[3]; p.wo[0] = d_in[4];
  p.x[1] = d_in[5]; p.k[1] = d_in[6]; p.v[1] = d_in[7]; p.wq[1] = d_in[8]; p.wo[1] = d_in[9];
  p.out = d_out; p.ws = d_ws;
  fused_attn_kernel<<<256, 512, 0, stream>>>(p);
}
```

```cpp
#include <hip/hip_runtime.h>
#include <stdint.h>
#pragma clang diagnostic ignored "-Wint-to-pointer-cast"

#define T_KB 466
#define T_START 800
constexpr int GATHER_W = 2;
constexpr int W_EARLY = 4;
constexpr int T0_MAX = 150;
constexpr int WO_AT = 80;
constexpr int D_POLL1 = 8;
constexpr int D_POLL2 = 20;
#define MAP5(a, b, c, d, e) ((u64)(a) | (u64)(b) << 5 | (u64)(c) << 10 | (u64)(d) << 15 | (u64)(e) << 20)
#define MAP_B (MAP5(12, 13, 19, 10, 11) | MAP5(15, 18, 14, 17, 16) << 25)
#define MAP_A (MAP5(12, 13, 17, 11, 16) | MAP5(10, 14, 15, 0, 0) << 25)
#define XW 8098054u
#define XI 34u

typedef unsigned u32;
typedef unsigned long long u64;
typedef u32 u32x2 __attribute__((ext_vector_type(2)));
typedef u32 u32x4 __attribute__((ext_vector_type(4)));
typedef float f32x2 __attribute__((ext_vector_type(2)));
typedef float f32x4 __attribute__((ext_vector_type(4)));
typedef __bf16 bf16x2 __attribute__((ext_vector_type(2)));
typedef __bf16 bf16x8 __attribute__((ext_vector_type(8)));
typedef short s16x4 __attribute__((ext_vector_type(4)));
typedef short s16x8 __attribute__((ext_vector_type(8)));
#define LAS __attribute__((address_space(3)))
#define GAS __attribute__((address_space(1)))
typedef __amdgpu_buffer_rsrc_t rsrc_t;

constexpr u32 KV_BYTES = 8u * 8u * 4096u * 128u * 2u;
constexpr u32 W_BYTES = 512u * 4096u * 2u;
constexpr u32 X_BYTES = 8u * 512u * 2u;
constexpr u32 WS_EPOCH = 0;
constexpr u32 WS_T0 = 256;
constexpr u32 WS_Q = 4096;
constexpr u32 REC = 72u * 16u;
constexpr u32 WS_O = WS_Q + 2u * 8u * 1024u * 16u;
constexpr u32 WS_Y = WS_O + 2u * 32u * 8u * 4u * REC;
constexpr u32 WS_END = WS_Y + 2u * 8u * 16u * 32u * 16u * 16u;
constexpr u32 L_RING = 0;
constexpr u32 L_W = 131072;
constexpr u32 L_RED = L_W;
constexpr u32 L_QF = L_W + 8192;
constexpr u32 L_QIMG = L_QF + 64;
constexpr u32 L_FLAG = L_RING + 8192 + 4096;
constexpr u32 L_OIMG = L_RING + 8192 + 4608;
constexpr u32 L_END = L_W + 32768;
constexpr u32 PSTR = 528;
constexpr float SC_LOG2E = 0.08838834764831845f * 1.4426950408889634f;
constexpr u32 SPIN_MAX = 1u << 15;

struct Params {
  const void* x[2];
  const void* k[2];
  const void* v[2];
  const void* wq[2];
  const void* wo[2];
  void* out;
  void* ws;
};

__device__ __forceinline__ rsrc_t mk_rsrc(const void* p, u32 bytes) {
  return __builtin_amdgcn_make_buffer_rsrc((void*)p, 0, bytes, 0x00020000);
}
__device__ __forceinline__ void dma4(rsrc_t r, u32 m, u32 v0, u32 v1, u32 v2, u32 v3, u32 s0, u32 s1, u32 s2, u32 s3) {
  asm volatile("s_mov_b32 m0, %[m]\n\ts_nop 0\n\t"
               "buffer_load_dwordx4 %[v0], %[r], %[s0] offen offset:0 nt lds\n\t"
               "buffer_load_dwordx4 %[v1], %[r], %[s1] offen offset:1024 nt lds\n\t"
               "buffer_load_dwordx4 %[v2], %[r], %[s2] offen offset:2048 nt lds\n\t"
               "buffer_load_dwordx4 %[v3], %[r], %[s3] offen offset:3072 nt lds"
               :: [m] "s"(m), [r] "s"(r), [v0] "v"(v0), [v1] "v"(v1), [v2] "v"(v2), [v3] "v"(v3),
                  [s0] "s"(s0), [s1] "s"(s1), [s2] "s"(s2), [s3] "s"(s3) : "memory");
}
__device__ __forceinline__ void dma1(rsrc_t r, u32 m, u32 v, u32 s) {
  asm volatile("s_mov_b32 m0, %[m]\n\ts_nop 0\n\tbuffer_load_dwordx4 %[v], %[r], %[s] offen lds"
               :: [m] "s"(m), [r] "s"(r), [v] "v"(v), [s] "s"(s) : "memory");
}
__device__ __forceinline__ void poll4t(u32x4 (&d)[5], rsrc_t r, u32 va, u32 vb, u32 vc, u32 t1, u32 t2) {
  u64 sv, t[10];
  u32 cnt;
  asm volatile("s_mov_b64 %[sv], exec\n\t"
               "s_mov_b64 %[m0], -1\n\t"
               "s_mov_b64 %[m1], -1\n\t"
               "s_mov_b64 %[m2], -1\n\t"
               "s_mov_b64 %[m3], -1\n\t"
               "s_mov_b64 %[m4], 0xff\n\t"
               "s_mov_b32 %[cnt], %[lim]\n"
               "1:\n\t"
               "s_mov_b64 exec, %[m0]\n\tbuffer_load_dwordx4 v[200:203], %[va], %[r], 0 offen sc1\n\t"
               "s_mov_b64 exec, %[m1]\n\tbuffer_load_dwordx4 v[204:207], %[va], %[r], 0 offen offset:1024 sc1\n\t"
               "s_mov_b64 exec, %[m2]\n\tbuffer_load_dwordx4 v[208:211], %[vb], %[r], 0 offen sc1\n\t"
               "s_mov_b64 exec, %[m3]\n\tbuffer_load_dwordx4 v[212:215], %[vb], %[r], 0 offen offset:1024 sc1\n\t"
               "s_mov_b64 exec, %[m4]\n\tbuffer_load_dwordx4 v[216:219], %[vc], %[r], 0 offen sc1\n\t"
               "s_mov_b64 exec, %[sv]\n\ts_waitcnt vmcnt(0)\n\t"
               "v_cmp_ne_u32_e64 %[m0], %[t1], v201\n\tv_cmp_ne_u32_e64 %[x0], %[t2], v203\n\t"
               "v_cmp_ne_u32_e64 %[m1], %[t1], v205\n\tv_cmp_ne_u32_e64 %[x1], %[t2], v207\n\t"
               "v_cmp_ne_u32_e64 %[m2], %[t1], v209\n\tv_cmp_ne_u32_e64 %[x2], %[t2], v211\n\t"
               "v_cmp_ne_u32_e64 %[m3], %[t1], v213\n\tv_cmp_ne_u32_e64 %[x3], %[t2], v215\n\t"
               "v_cmp_ne_u32_e64 %[m4], %[t1], v217\n\tv_cmp_ne_u32_e64 %[x4], %[t2], v219\n\t"
               "s_or_b64 %[m0], %[m0], %[x0]\n\t"
               "s_or_b64 %[m1], %[m1], %[x1]\n\t"
               "s_or_b64 %[m2], %[m2], %[x2]\n\t"
               "s_or_b64 %[m3], %[m3], %[x3]\n\t"
               "s_or_b64 %[m4], %[m4], %[x4]\n\t"
               "s_and_b64 %[m4], %[m4], 0xff\n\t"
               "s_or_b64 %[x0], %[m0], %[m1]\n\t"
               "s_or_b64 %[x0], %[x0], %[m2]\n\t"
               "s_or_b64 %[x0], %[x0], %[m3]\n\t"
               "s_or_b64 %[x0], %[x0], %[m4]\n\t"
               "s_cbranch_scc0 2f\n\t"
               "s_sub_u32 %[cnt], %[cnt], 1\n\ts_cbranch_scc0 1b\n"
               "2:"
               : "=&{v[200:203]}"(d[0]), "=&{v[204:207]}"(d[1]), "=&{v[208:211]}"(d[2]), "=&{v[212:215]}"(d[3]),
                 "=&{v[216:219]}"(d[4]), [sv] "=&s"(sv), [cnt] "=&s"(cnt), [m0] "=&s"(t[0]),
                 [m1] "=&s"(t[1]), [m2] "=&s"(t[2]), [m3] "=&s"(t[3]), [m4] "=&s"(t[4]),
                 [x0] "=&s"(t[5]), [x1] "=&s"(t[6]), [x2] "=&s"(t[7]), [x3] "=&s"(t[8]),
                 [x4] "=&s"(t[9])
               : [va] "v"(va), [vb] "v"(vb), [vc] "v"(vc), [r] "s"(r), [t1] "s"(t1), [t2] "s"(t2), [lim] "n"(SPIN_MAX)
               : "memory", "scc");
}
__device__ __forceinline__ void poll5(u32x4 (&d)[5], rsrc_t r, u32 va, u32 vb, u32 t1, u32 t2) {
  u64 sv, t[10];
  u32 cnt;
  asm volatile("s_mov_b64 %[sv], exec\n\t"
               "s_mov_b64 %[m0], -1\n\t"
               "s_mov_b64 %[m1], -1\n\t"
               "s_mov_b64 %[m2], -1\n\t"
               "s_mov_b64 %[m3], -1\n\t"
               "s_mov_b64 %[m4], 15\n\t"
               "s_mov_b32 %[cnt], %[lim]\n"
               "1:\n\t"
               "s_mov_b64 exec, %[m0]\n\tbuffer_load_dwordx4 v[200:203], %[va], %[r], 0 offen sc1\n\t"
               "s_mov_b64 exec, %[m1]\n\tbuffer_load_dwordx4 v[204:207], %[va], %[r], 0 offen offset:1152 sc1\n\t"
               "s_mov_b64 exec, %[m2]\n\tbuffer_load_dwordx4 v[208:211], %[va], %[r], 0 offen offset:2304 sc1\n\t"
               "s_mov_b64 exec, %[m3]\n\tbuffer_load_dwordx4 v[212:215], %[va], %[r], 0 offen offset:3456 sc1\n\t"
               "s_mov_b64 exec, %[m4]\n\tbuffer_load_dwordx4 v[216:219], %[vb], %[r], 0 offen sc1\n\t"
               "s_mov_b64 exec, %[sv]\n\ts_waitcnt vmcnt(0)\n\t"
               "v_cmp_ne_u32_e64 %[m0], %[t1], v201\n\tv_cmp_ne_u32_e64 %[x0], %[t2], v203\n\t"
               "v_cmp_ne_u32_e64 %[m1], %[t1], v205\n\tv_cmp_ne_u32_e64 %[x1], %[t2], v207\n\t"
               "v_cmp_ne_u32_e64 %[m2], %[t1], v209\n\tv_cmp_ne_u32_e64 %[x2], %[t2], v211\n\t"
               "v_cmp_ne_u32_e64 %[m3], %[t1], v213\n\tv_cmp_ne_u32_e64 %[x3], %[t2], v215\n\t"
               "v_cmp_ne_u32_e64 %[m4], %[t1], v217\n\tv_cmp_ne_u32_e64 %[x4], %[t2], v219\n\t"
               "s_or_b64 %[m0], %[m0], %[x0]\n\t"
               "s_or_b64 %[m1], %[m1], %[x1]\n\t"
               "s_or_b64 %[m2], %[m2], %[x2]\n\t"
               "s_or_b64 %[m3], %[m3], %[x3]\n\t"
               "s_or_b64 %[m4], %[m4], %[x4]\n\t"
               "s_and_b64 %[m4], %[m4], 15\n\t"
               "s_or_b64 %[x0], %[m0], %[m1]\n\t"
               "s_or_b64 %[x0], %[x0], %[m2]\n\t"
               "s_or_b64 %[x0], %[x0], %[m3]\n\t"
               "s_or_b64 %[x0], %[x0], %[m4]\n\t"
               "s_cbranch_scc0 2f\n\t"
               "s_sub_u32 %[cnt], %[cnt], 1\n\ts_cbranch_scc0 1b\n"
               "2:"
               : "=&{v[200:203]}"(d[0]), "=&{v[204:207]}"(d[1]), "=&{v[208:211]}"(d[2]), "=&{v[212:215]}"(d[3]),
                 "=&{v[216:219]}"(d[4]), [sv] "=&s"(sv), [cnt] "=&s"(cnt), [m0] "=&s"(t[0]),
                 [m1] "=&s"(t[1]), [m2] "=&s"(t[2]), [m3] "=&s"(t[3]), [m4] "=&s"(t[4]),
                 [x0] "=&s"(t[5]), [x1] "=&s"(t[6]), [x2] "=&s"(t[7]), [x3] "=&s"(t[8]),
                 [x4] "=&s"(t[9])
               : [va] "v"(va), [vb] "v"(vb), [r] "s"(r), [t1] "s"(t1), [t2] "s"(t2), [lim] "n"(SPIN_MAX)
               : "memory", "scc");
}
__device__ __forceinline__ void poll8(u32x4 (&d)[8], rsrc_t r, u32 va, u32 vb, u32 t1, u32 t2) {
  u64 sv, t[16];
  u32 cnt;
  asm volatile("s_mov_b64 %[sv], exec\n\t"
               "s_mov_b64 %[m0], -1\n\t"
               "s_mov_b64 %[m1], -1\n\t"
               "s_mov_b64 %[m2], -1\n\t"
               "s_mov_b64 %[m3], -1\n\t"
               "s_mov_b64 %[m4], -1\n\t"
               "s_mov_b64 %[m5], -1\n\t"
               "s_mov_b64 %[m6], -1\n\t"
               "s_mov_b64 %[m7], -1\n\t"
               "s_mov_b32 %[cnt], %[lim]\n"
               "1:\n\t"
               "s_mov_b64 exec, %[m0]\n\tbuffer_load_dwordx4 v[200:203], %[va], %[r], 0 offen sc1\n\t"
               "s_mov_b64 exec, %[m1]\n\tbuffer_load_dwordx4 v[204:207], %[va], %[r], 0 offen offset:1024 sc1\n\t"
               "s_mov_b64 exec, %[m2]\n\tbuffer_load_dwordx4 v[208:211], %[va], %[r], 0 offen offset:2048 sc1\n\t"
               "s_mov_b64 exec, %[m3]\n\tbuffer_load_dwordx4 v[212:215], %[va], %[r], 0 offen offset:3072 sc1\n\t"
               "s_mov_b64 exec, %[m4]\n\tbuffer_load_dwordx4 v[216:219], %[vb], %[r], 0 offen sc1\n\t"
               "s_mov_b64 exec, %[m5]\n\tbuffer_load_dwordx4 v[220:223], %[vb], %[r], 0 offen offset:1024 sc1\n\t"
               "s_mov_b64 exec, %[m6]\n\tbuffer_load_dwordx4 v[224:227], %[vb], %[r], 0 offen offset:2048 sc1\n\t"
               "s_mov_b64 exec, %[m7]\n\tbuffer_load_dwordx4 v[228:231], %[vb], %[r], 0 offen offset:3072 sc1\n\t"
               "s_mov_b64 exec, %[sv]\n\ts_waitcnt vmcnt(0)\n\t"
               "v_cmp_ne_u32_e64 %[m0], %[t1], v201\n\tv_cmp_ne_u32_e64 %[x0], %[t2], v203\n\t"
               "v_cmp_ne_u32_e64 %[m1], %[t1], v205\n\tv_cmp_ne_u32_e64 %[x1], %[t2], v207\n\t"
               "v_cmp_ne_u32_e64 %[m2], %[t1], v209\n\tv_cmp_ne_u32_e64 %[x2], %[t2], v211\n\t"
               "v_cmp_ne_u32_e64 %[m3], %[t1], v213\n\tv_cmp_ne_u32_e64 %[x3], %[t2], v215\n\t"
               "v_cmp_ne_u32_e64 %[m4], %[t1], v217\n\tv_cmp_ne_u32_e64 %[x4], %[t2], v219\n\t"
               "v_cmp_ne_u32_e64 %[m5], %[t1], v221\n\tv_cmp_ne_u32_e64 %[x5], %[t2], v223\n\t"
               "v_cmp_ne_u32_e64 %[m6], %[t1], v225\n\tv_cmp_ne_u32_e64 %[x6], %[t2], v227\n\t"
               "v_cmp_ne_u32_e64 %[m7], %[t1], v229\n\tv_cmp_ne_u32_e64 %[x7], %[t2], v231\n\t"
               "s_or_b64 %[m0], %[m0], %[x0]\n\t"
               "s_or_b64 %[m1], %[m1], %[x1]\n\t"
               "s_or_b64 %[m2], %[m2], %[x2]\n\t"
               "s_or_b64 %[m3], %[m3], %[x3]\n\t"
               "s_or_b64 %[m4], %[m4], %[x4]\n\t"
               "s_or_b64 %[m5], %[m5], %[x5]\n\t"
               "s_or_b64 %[m6], %[m6], %[x6]\n\t"
               "s_or_b64 %[m7], %[m7], %[x7]\n\t"
               "s_or_b64 %[x0], %[m0], %[m1]\n\t"
               "s_or_b64 %[x0], %[x0], %[m2]\n\t"
               "s_or_b64 %[x0], %[x0], %[m3]\n\t"
               "s_or_b64 %[x0], %[x0], %[m4]\n\t"
               "s_or_b64 %[x0], %[x0], %[m5]\n\t"
               "s_or_b64 %[x0], %[x0], %[m6]\n\t"
               "s_or_b64 %[x0], %[x0], %[m7]\n\t"
               "s_cbranch_scc0 2f\n\t"
               "s_sub_u32 %[cnt], %[cnt], 1\n\ts_cbranch_scc0 1b\n"
               "2:"
               : "=&{v[200:203]}"(d[0]), "=&{v[204:207]}"(d[1]), "=&{v[208:211]}"(d[2]), "=&{v[212:215]}"(d[3]),
                 "=&{v[216:219]}"(d[4]), "=&{v[220:223]}"(d[5]), "=&{v[224:227]}"(d[6]), "=&{v[228:231]}"(d[7]),
                 [sv] "=&s"(sv), [cnt] "=&s"(cnt), [m0] "=&s"(t[0]), [m1] "=&s"(t[1]),
                 [m2] "=&s"(t[2]), [m3] "=&s"(t[3]), [m4] "=&s"(t[4]), [m5] "=&s"(t[5]),
                 [m6] "=&s"(t[6]), [m7] "=&s"(t[7]), [x0] "=&s"(t[8]), [x1] "=&s"(t[9]),
                 [x2] "=&s"(t[10]), [x3] "=&s"(t[11]), [x4] "=&s"(t[12]), [x5] "=&s"(t[13]),
                 [x6] "=&s"(t[14]), [x7] "=&s"(t[15])
               : [va] "v"(va), [vb] "v"(vb), [r] "s"(r), [t1] "s"(t1), [t2] "s"(t2), [lim] "n"(SPIN_MAX)
               : "memory", "scc");
}
__device__ __forceinline__ void pin(u32& v) { asm volatile("" : "+v"(v)); }
__device__ __forceinline__ float lane_f(u32 v, int l) { return __uint_as_float(__builtin_amdgcn_readlane(v, l)); }
__device__ __forceinline__ void st16_sc1(u32x4 d, rsrc_t r, u32 v) {
  __builtin_amdgcn_raw_buffer_store_b128(d, r, v, 0, 16);
}
template <int N> __device__ __forceinline__ void wait_vm() { asm volatile("s_waitcnt vmcnt(%0)" :: "n"(N) : "memory"); }
__device__ __forceinline__ void wait_lgkm() { asm volatile("s_waitcnt lgkmcnt(0)" ::: "memory"); }
__device__ __forceinline__ void bar() { asm volatile("s_waitcnt lgkmcnt(0)\n\ts_barrier" ::: "memory"); }

__device__ __forceinline__ u32x4 lds_r128(u32 a) { return *(const LAS u32x4*)a; }
__device__ __forceinline__ f32x4 lds_rf4(u32 a) { return *(const LAS f32x4*)a; }
__device__ __forceinline__ f32x2 lds_rf2(u32 a) { return *(const LAS f32x2*)a; }
__device__ __forceinline__ s16x4 lds_tr(u32 a) { return __builtin_amdgcn_ds_read_tr16_b64_v4i16((LAS s16x4*)a); }
__device__ __forceinline__ bf16x8 cat_tr(s16x4 a, s16x4 b) {
  return __builtin_bit_cast(bf16x8, __builtin_shufflevector(a, b, 0, 1, 2, 3, 4, 5, 6, 7));
}
__device__ __forceinline__ u32 pk_bf16(float lo, float hi) {
  f32x2 f = {lo, hi};
  return __builtin_bit_cast(u32, __builtin_convertvector(f, bf16x2));
}
__device__ __forceinline__ f32x4 mfma(bf16x8 a, bf16x8 b, f32x4 c) { return __builtin_amdgcn_mfma_f32_16x16x32_bf16(a, b, c, 0, 0, 0); }
template <int CTRL> __device__ __forceinline__ float dpp(float x) {
  return __uint_as_float(__builtin_amdgcn_update_dpp(0u, __float_as_uint(x), CTRL, 0xf, 0xf, true));
}
__device__ __forceinline__ float vmax(float a, float b) {
  float r; asm("v_max_f32 %0, %1, %2" : "=v"(r) : "v"(a), "v"(b)); return r;
}
__device__ __forceinline__ float quad_max(float x) {
  asm("s_nop 1\n\tv_max_f32_dpp %0, %0, %0 quad_perm:[1,0,3,2] row_mask:0xf bank_mask:0xf\n\t"
      "s_nop 1\n\tv_max_f32_dpp %0, %0, %0 quad_perm:[2,3,0,1] row_mask:0xf bank_mask:0xf" : "+v"(x));
  return x;
}
__device__ __forceinline__ float fold32(float a, float b) {
  auto s = __builtin_amdgcn_permlane32_swap(__float_as_uint(a), __float_as_uint(b), false, false);
  return __uint_as_float(s[0]) + __uint_as_float(s[1]);
}
__device__ __forceinline__ float xrow_max(float x) {
  auto s = __builtin_amdgcn_permlane16_swap(__float_as_uint(x), __float_as_uint(x), false, false);
  x = fmaxf(__uint_as_float(s[0]), __uint_as_float(s[1]));
  auto t = __builtin_amdgcn_permlane32_swap(__float_as_uint(x), __float_as_uint(x), false, false);
  return fmaxf(__uint_as_float(t[0]), __uint_as_float(t[1]));
}
__device__ __forceinline__ float xrow_sum(float x) {
  auto s = __builtin_amdgcn_permlane16_swap(__float_as_uint(x), __float_as_uint(x), false, false);
  x = __uint_as_float(s[0]) + __uint_as_float(s[1]);
  auto t = __builtin_amdgcn_permlane32_swap(__float_as_uint(x), __float_as_uint(x), false, false);
  return __uint_as_float(t[0]) + __uint_as_float(t[1]);
}

struct Soft { float m, l; f32x4 acc[8]; };

__device__ __forceinline__ void load_k(u32x4 (&kf)[2][4], const u32 (&ka)[4]) {
#pragma unroll
  for (int u = 0; u < 2; ++u)
#pragma unroll
    for (int kk = 0; kk < 4; ++kk) kf[u][kk] = lds_r128(ka[kk] + u * 4096);
  wait_lgkm();
}
__device__ __forceinline__ void load_v(s16x4 (&vf)[8][2], const u32 (&va)[8]) {
#pragma unroll
  for (int dt = 0; dt < 8; ++dt) {
    vf[dt][0] = lds_tr(va[dt]);
    vf[dt][1] = lds_tr(va[dt] + 4096);
  }
  wait_lgkm();
}
template <bool FIRST>
__device__ __forceinline__ bf16x8 scores(Soft& st, const u32x4 (&kf)[2][4], const bf16x8 (&qf)[4]) {
  f32x4 s0 = {0.f, 0.f, 0.f, 0.f}, s1 = {0.f, 0.f, 0.f, 0.f};
#pragma unroll
  for (int kk = 0; kk < 4; ++kk) {
    s0 = mfma(__builtin_bit_cast(bf16x8, kf[0][kk]), qf[kk], s0);
    s1 = mfma(__builtin_bit_cast(bf16x8, kf[1][kk]), qf[kk], s1);
  }
  float mx = fmaxf(fmaxf(fmaxf(s0[0], s0[1]), fmaxf(s0[2], s0[3])), fmaxf(fmaxf(s1[0], s1[1]), fmaxf(s1[2], s1[3])));
  mx = xrow_max(mx);
  const float mn = FIRST ? mx : fmaxf(st.m, mx);
  const float mc = mn * SC_LOG2E;
  float pr[8];
#pragma unroll
  for (int j = 0; j < 4; ++j) {
    pr[j] = __builtin_amdgcn_exp2f(__builtin_fmaf(s0[j], SC_LOG2E, -mc));
    pr[4 + j] = __builtin_amdgcn_exp2f(__builtin_fmaf(s1[j], SC_LOG2E, -mc));
  }
  const float ps = ((pr[0] + pr[1]) + (pr[2] + pr[3])) + ((pr[4] + pr[5]) + (pr[6] + pr[7]));
  u32x4 pk = {pk_bf16(pr[0], pr[1]), pk_bf16(pr[2], pr[3]), pk_bf16(pr[4], pr[5]), pk_bf16(pr[6], pr[7])};
  if (FIRST) {
    st.l = ps;
  } else {
    const float alpha = __builtin_amdgcn_exp2f(st.m * SC_LOG2E - mc);
    st.l = __builtin_fmaf(st.l, alpha, ps);
#pragma unroll
    for (int dt = 0; dt < 8; ++dt) st.acc[dt] *= alpha;
  }
  st.m = mn;
  return __builtin_bit_cast(bf16x8, pk);
}
template <bool FIRST>
__device__ __forceinline__ void accumulate(Soft& st, const s16x4 (&vf)[8][2], bf16x8 pf) {
#pragma unroll
  for (int dt = 0; dt < 8; ++dt) {
    const f32x4 z = {0.f, 0.f, 0.f, 0.f};
    st.acc[dt] = mfma(cat_tr(vf[dt][0], vf[dt][1]), pf, FIRST ? z : st.acc[dt]);
  }
}
template <int WAIT_K, int WAIT_V, bool FIRST, class FK, class FV>
__device__ __forceinline__ void attn_block(Soft& st, const bf16x8 (&qf)[4], const u32 (&ka)[4], const u32 (&va)[8], FK issue_k, FV issue_v) {
  wait_vm<WAIT_K>();
  u32x4 kf[2][4];
  load_k(kf, ka);
  issue_k();
  const bf16x8 pf = scores<FIRST>(st, kf, qf);
  wait_vm<WAIT_V>();
  s16x4 vf[8][2];
  load_v(vf, va);
  issue_v();
  accumulate<FIRST>(st, vf, pf);
}

__global__ void __launch_bounds__(512, 2) fused_attn_kernel(Params p) {
  __shared__ __attribute__((aligned(16))) char smem[L_END];
  const u32 lds0 = (u32)(uintptr_t)(LAS char*)smem;
  const int tid = threadIdx.x, lane = tid & 63;
  const int w = __builtin_amdgcn_readfirstlane(tid >> 6);
  const int g = lane >> 4, n = lane & 15;
  const int wg = blockIdx.x, xc = wg & 7, slot = wg >> 3;
  const u32 t_wave = (u32)__builtin_amdgcn_s_memrealtime();
  const void *px0 = p.x[0], *px1 = p.x[1], *pk0 = p.k[0], *pk1 = p.k[1], *pv0 = p.v[0], *pv1 = p.v[1];
  const void *pq0 = p.wq[0], *pq1 = p.wq[1], *po0 = p.wo[0], *po1 = p.wo[1];
  void *pout = p.out, *pws = p.ws;
  asm volatile("" : "+s"(px0), "+s"(px1), "+s"(pk0), "+s"(pk1), "+s"(pv0), "+s"(pv1));
  asm volatile("" : "+s"(pq0), "+s"(pq1), "+s"(po0), "+s"(po1), "+s"(pout), "+s"(pws));

  const u32 kslot = lds0 + L_RING + w * 16384, vslot = kslot + 8192;
  const int br2 = slot >> 4;

  const int tb = xc, th = slot >> 2, tj = slot & 3;
  const u32 pair_off = (u32)(tb * 8 + th) * (4096u * 256u);
  constexpr u64 mapb = MAP_B, mapa = MAP_A;
  constexpr u32 xw = XW, xi = XI;
  auto put = [](u64 map, int k, u32 bit) { return (bit & 1) << ((u32)(map >> (5 * k)) & 31); };
  u32 wr = w, ir = 0;
  {
    const u32 id = tj | th << 2 | tb << 5;
#pragma unroll
    for (int k = 0; k < 8; ++k) {
      if ((id >> k) & 1) { wr ^= (xw >> (3 * k)) & 7; ir ^= (xi >> (2 * k)) & 3; }
    }
  }
  const u32 sA = pair_off + 3072u * 256u + put(mapa, 3, wr) + put(mapa, 4, wr >> 1) + put(mapa, 5, wr >> 2) + put(mapa, 6, tj) + put(mapa, 7, tj >> 1);
  const u32 sB = pair_off + put(mapb, 3, wr) + put(mapb, 4, wr >> 1) + put(mapb, 5, wr >> 2) + put(mapb, 8, tj) + put(mapb, 9, tj >> 1);
  auto blk = [&](u32 i) { return sB + put(mapb, 6, i ^ ir) + put(mapb, 7, (i ^ ir) >> 1); };
  u32 kv[4], vv[2];
#pragma unroll
  for (int t = 0; t < 4; ++t) kv[t] = g * 256 + 16 * (n ^ ((4 * t + g) & 15));
#pragma unroll
  for (int t = 0; t < 2; ++t) vv[t] = g * 256 + 16 * (n ^ (((4 * t + g) & 7) << 1));
  const rsrc_t rkA = mk_rsrc(pk0, KV_BYTES), rvA = mk_rsrc(pv0, KV_BYTES);
  const rsrc_t rkB = mk_rsrc(pk1, KV_BYTES), rvB = mk_rsrc(pv1, KV_BYTES);
  auto issue_kv = [&](rsrc_t r, u32 slotaddr, u32 s, bool isv, u64 map) {
    const u32 d1 = put(map, 0, 1) - 1024, d2 = put(map, 1, 1) - 2048, s2 = s + put(map, 2, 1);
    if (isv) {
      dma4(r, slotaddr, vv[0], vv[1], vv[0], vv[1], s, s + d1, s + d2, s + d1 + d2);
      dma4(r, slotaddr + 4096, vv[0], vv[1], vv[0], vv[1], s2, s2 + d1, s2 + d2, s2 + d1 + d2);
    } else {
      dma4(r, slotaddr, kv[0], kv[1], kv[2], kv[3], s, s + d1, s + d2, s + d1 + d2);
      dma4(r, slotaddr + 4096, kv[0], kv[1], kv[2], kv[3], s2, s2 + d1, s2 + d2, s2 + d1 + d2);
    }
  };
  const int cg = xc * 16 + (slot & 15);
  {
    const rsrc_t rx = mk_rsrc(br2 ? px1 : px0, X_BYTES), rw = mk_rsrc(br2 ? pq1 : pq0, W_BYTES);
    const u32 wv = (lane >> 2) * 8192 + 16 * ((lane & 3) ^ (((lane >> 5) & 1) << 1));
    const u32 ws0 = (u32)w * (64u * 8192u) + cg * 64;
    dma1(rx, lds0 + L_RED + w * 1024, (lane >> 3) * 1024 + (lane & 7) * 16, w * 128);
    dma4(rw, vslot, wv, wv, wv, wv, ws0, ws0 + (131072 - 1024), ws0 + 2 * (131072 - 1024), ws0 + 3 * (131072 - 1024));
  }
  u64 ep;
  asm volatile("s_load_dwordx2 %0, %1, 0x0" : "=s"(ep) : "s"(pws) : "memory");
  if (lane == 0) *(LAS u32*)(lds0 + L_FLAG + w * 4) = 0;
  if (tid < 2) *(LAS u32*)(lds0 + L_QF + tid * 4) = 0;
  bar();
  asm volatile("" : "+s"(ep));
  ep += 1;
  const u32 t1 = (u32)ep, t2 = (u32)ep ^ (u32)(ep >> 32);
  const rsrc_t rws = mk_rsrc(pws, WS_END);

  const int phase = (w + 7) & 7;
  constexpr u32 tkb = T_KB, tstart = T_START;
  const u32 t_off = (tstart + (u32)phase * tkb) >> 4;
  u32 t_base = t_wave + t_off;
  const int oh = xc * 4 + ((slot >> 2) & 3), ocg = slot & 3;
  auto issue_wo = [&](int r16) {
    const rsrc_t rwo = mk_rsrc(br2 ? po1 : po0, W_BYTES);
    const u32 v0 = g * 1024 + 16 * (n ^ ((g & 7) << 1)), v1 = g * 1024 + 16 * (n ^ (((4 + g) & 7) << 1));
    const u32 s = (u32)(oh * 128 + r16 * 16) * 1024u + ocg * 256;
    dma4(rwo, lds0 + L_W + r16 * 4096, v0, v1, v0, v1, s, s + 3072, s + 2 * 3072, s + 3 * 3072);
  };
  auto pace = [&](int kb, u32 extra = 0) {
    const u32 due = t_base + extra + (((u32)kb * tkb) >> 4);
#pragma nounroll
    for (u32 spin = 0; spin < SPIN_MAX; ++spin) {
      if ((int)((u32)__builtin_amdgcn_s_memrealtime() - due) >= 0) break;
      __builtin_amdgcn_s_sleep(1);
    }
  };
  auto gather_q = [&]() {
    u32x4 d[5];
    const u32 qoff = WS_Q + (tb * 1024 + th * 128 + lane) * 16;
    poll4t(d, rws, qoff, qoff + 8 * 1024 * 16, WS_T0 + (lane & 7) * 16, t1, t2);
    int late = 0;
#pragma unroll
    for (int i = 0; i < 8; ++i) late = max(late, (int)(t_wave - __builtin_amdgcn_readlane(d[4][0], i)));
    late = min(late, T0_MAX);
    if (lane == 0) *(LAS u32*)(lds0 + L_QF + 8) = t_wave - (u32)late;
#pragma unroll
    for (int i = 0; i < 4; ++i) {
      const int idx = (i & 1) * 64 + lane;
      u32x2 qv = {d[i][0], d[i][2]};
      *(LAS u32x2*)(lds0 + L_QIMG + ((i >> 1) * 4 + (idx >> 5)) * 272 + (idx & 31) * 8) = qv;
    }
    wait_lgkm();
    if (lane == 0) *(volatile LAS u32*)(lds0 + L_QF) = 1;
  };
  if (phase < W_EARLY) { pace(0); issue_kv(rkA, kslot, sA, false, mapa); wait_vm<8>(); }
  else wait_vm<0>();
  {
    f32x4 qa[2] = {{0.f, 0.f, 0.f, 0.f}, {0.f, 0.f, 0.f, 0.f}};
    const int q = (lane >> 2) & 3, pp = lane & 3;
    const u32x4 xf0 = lds_r128(lds0 + L_RED + w * 1024 + (n & 7) * 128 + g * 16);
    const u32x4 xf1 = lds_r128(lds0 + L_RED + w * 1024 + (n & 7) * 128 + 64 + g * 16);
    s16x4 wf[2][2][2];
#pragma unroll
    for (int kk = 0; kk < 2; ++kk)
#pragma unroll
      for (int ct = 0; ct < 2; ++ct)
#pragma unroll
        for (int u = 0; u < 2; ++u)
          wf[kk][ct][u] = lds_tr(vslot + (32 * kk + 8 * g + 4 * u + q) * 64 + 16 * ((2 * ct + (pp >> 1)) ^ ((g & 1) << 1)) + 8 * (pp & 1));
#pragma unroll
    for (int ct = 0; ct < 2; ++ct) {
      qa[ct] = mfma(cat_tr(wf[0][ct][0], wf[0][ct][1]), __builtin_bit_cast(bf16x8, xf0), qa[ct]);
      qa[ct] = mfma(cat_tr(wf[1][ct][0], wf[1][ct][1]), __builtin_bit_cast(bf16x8, xf1), qa[ct]);
    }
    const f32x4 qs = n < 8 ? qa[0] : qa[1];
    wait_lgkm();
    *(LAS f32x4*)(lds0 + L_RED + (w * 256 + (n & 7) * 32 + (n >> 3) * 16 + 4 * g) * 4) = qs;
    wait_lgkm();
    if (lane == 0) *(volatile LAS u32*)(lds0 + L_FLAG + w * 4) = 1;
  }
  if (w != 0) {
    if (phase >= W_EARLY) { pace(0); issue_kv(rkA, kslot, sA, false, mapa); }
  } else {
    for (u32 spin = 0; spin < SPIN_MAX * 8; ++spin) {
      const u32x4 f0 = *(volatile LAS u32x4*)(lds0 + L_FLAG), f1 = *(volatile LAS u32x4*)(lds0 + L_FLAG + 16);
      if ((f0[0] & f0[1] & f0[2] & f0[3] & f1[0] & f1[1] & f1[2] & f1[3]) == 1) break;
    }
    const int b = lane >> 3, c4 = lane & 7;
    f32x4 s = lds_rf4(lds0 + L_RED + (b * 32 + c4 * 4) * 4);
#pragma unroll
    for (int i = 1; i < 8; ++i) s += lds_rf4(lds0 + L_RED + (i * 256 + b * 32 + c4 * 4) * 4);
    u32x4 u = {pk_bf16(s[0], s[1]), t1, pk_bf16(s[2], s[3]), t2};
    st16_sc1(u, rws, WS_Q + ((br2 * 8 + b) * 1024 + cg * 8 + c4) * 16);
    if (wg < 8 && lane == 0) st16_sc1(u32x4{t_wave, t1, t_wave, t2}, rws, WS_T0 + wg * 16);
    pace(0); issue_kv(rkA, kslot, sA, false, mapa);
  }
  pace(8); issue_kv(rvA, vslot, sA, true, mapa);

  {
    u32 ka[4], va[8];
#pragma unroll
    for (int kk = 0; kk < 4; ++kk) ka[kk] = kslot + n * 256 + 16 * ((4 * kk + g) ^ n);
    {
      const int q = (lane >> 2) & 3, pp = lane & 3, r = 4 * g + q;
#pragma unroll
      for (int dt = 0; dt < 8; ++dt) va[dt] = vslot + r * 256 + 32 * (dt ^ (r & 7)) + 8 * pp;
    }
    Soft sa, sb;
    bf16x8 qg[4];
    {
      u32x4 kf[2][4];
      s16x4 vf[8][2];
      wait_vm<8>();
      load_k(kf, ka);
      if (w == GATHER_W) gather_q();
      pace(16); issue_kv(rkB, kslot, blk(0), false, mapb);
      wait_vm<8>();
      load_v(vf, va);
      pace(24); issue_kv(rvB, vslot, blk(0), true, mapb);
      for (u32 spin = 0; spin < SPIN_MAX * 8; ++spin) {
        if (*(volatile LAS u32*)(lds0 + L_QF) == 1) break;
        __builtin_amdgcn_s_sleep(1);
      }
      asm volatile("" ::: "memory");
      t_base = __builtin_amdgcn_readfirstlane(*(const LAS u32*)(lds0 + L_QF + 8)) + t_off;
      bf16x8 qf[4];
#pragma unroll
      for (int kk = 0; kk < 4; ++kk) {
        qf[kk] = __builtin_bit_cast(bf16x8, lds_r128(lds0 + L_QIMG + (n & 3) * 272 + (32 * kk + 8 * g) * 2));
        qg[kk] = __builtin_bit_cast(bf16x8, lds_r128(lds0 + L_QIMG + (4 + (n & 3)) * 272 + (32 * kk + 8 * g) * 2));
      }
      wait_lgkm();
      if (lane == 0) __hip_atomic_fetch_add((LAS u32*)(lds0 + L_QF + 4), 1u, __ATOMIC_RELAXED, __HIP_MEMORY_SCOPE_WORKGROUP);
      const bf16x8 pf = scores<true>(sa, kf, qf);
      accumulate<true>(sa, vf, pf);
    }
    auto code_pf = [&]() {
      if (phase == 2) {
        u64 pc; int off, len;
        asm volatile("s_getpc_b64 %0\n.Lfa_here:\n\ts_mov_b32 %1, .Lfa_tail-.Lfa_here\n\ts_mov_b32 %2, .Lfa_end-.Lfa_tail"
                     : "=s"(pc), "=s"(off), "=s"(len));
        const rsrc_t rc = mk_rsrc((const void*)(pc + (long long)off), (u32)max(len, 0));
        asm volatile("s_mov_b32 m0, %[m]\n\ts_nop 0\n\tbuffer_load_dword %[v], %[r], 0 offen lds"
                     :: [m] "s"(lds0 + L_W + 16384), [r] "s"(rc), [v] "v"(lane * 128) : "memory");
      }
    };
    attn_block<8, 8, true>(sb, qg, ka, va,
                           [&]() { pace(32); issue_kv(rkB, kslot, blk(1), false, mapb); },
                           [&]() { pace(40); issue_kv(rvB, vslot, blk(1), true, mapb); });
    attn_block<8, 8, false>(sb, qg, ka, va,
                            [&]() { pace(48); issue_kv(rkB, kslot, blk(2), false, mapb); },
                            [&]() { pace(56); issue_kv(rvB, vslot, blk(2), true, mapb); });
    attn_block<8, 8, false>(sb, qg, ka, va,
                            [&]() { pace(64); code_pf(); issue_kv(rkB, kslot, blk(3), false, mapb); },
                            [&]() { pace(72); issue_kv(rvB, vslot, blk(3), true, mapb); });
    auto park = [&](const Soft& st, int br) {
      const float l = xrow_sum(st.l);
      if (n < 4) {
#pragma unroll
        for (int dt = 0; dt < 8; ++dt) *(LAS f32x4*)(kslot + (br * 4 + n) * PSTR + (16 * dt + 4 * g) * 4) = st.acc[dt];
        if (g == 0) *(LAS f32x2*)(kslot + 8 * PSTR + w * 64 + (br * 4 + n) * 8) = f32x2{st.m * SC_LOG2E, l};
      }
    };
    const int hi = lane >> 5, l5 = lane & 31;
    u32 a_ml = lds0 + L_RING + (4 * hi + (lane & 3)) * (16384 + 64) + 8 * PSTR + w * 8;
    u32 a_ov = lds0 + L_RING + 4 * hi * 16384 + w * PSTR + l5 * 16;
    u32 a_pub = WS_O + (u32)((((w >> 2) * 32 + th * 4 + (w & 3)) * 8 + tb) * 4 + tj) * REC + lane * 16;
    const u32 rid = ((br2 * 32 + oh) * 8 + w) * 4;
    u32 a_d = WS_O + rid * REC + lane * 16, a_e = WS_O + (rid + (lane & 3)) * REC + 1024;
    u32 a_oimg, a_w, a_of, a_y, y_src;
    {
      const int dd = 4 * l5 + 2 * hi, r = dd & 31;
      a_oimg = lds0 + L_OIMG + w * 272 + ((dd & ~31) + ((r & 15) >> 2) * 8 + (r >> 4) * 4 + (r & 3)) * 2;
      const int q = (lane >> 2) & 3, pp = lane & 3, rr = 4 * g + q;
      a_w = lds0 + L_W + rr * 256 + 32 * (w ^ (rr & 7)) + 8 * pp;
      a_of = lds0 + L_OIMG + (n & 7) * 272 + 8 * g * 2;
      const u32 yid = ((br2 * 8 + (lane >> 3)) * 16 + ocg * 4 + (w >> 1)) * 32 + oh;
      a_y = WS_Y + (yid * 16 + (w & 1) * 8 + (lane & 7)) * 16;
      y_src = (((lane & 7) >> 1) * 16 + (lane & 1) * 8 + (lane >> 3)) * 4;
    }
    attn_block<8, 0, false>(sb, qg, ka, va, [&]() {
      park(sa, 0);
      pin(a_ml); pin(a_ov); pin(a_pub); pin(a_d); pin(a_e); pin(a_oimg); pin(a_w); pin(a_of); pin(a_y); pin(y_src);
    }, [&]() {});
    asm volatile(".Lfa_tail:");
    park(sb, 1);
    if (phase < 4) {
      if (phase <= 1) {
        for (u32 spin = 0; spin < SPIN_MAX * 8; ++spin)
          if (*(volatile LAS u32*)(lds0 + L_QF + 4) == 8u + phase) break;
        if (phase == 0 && lane == 0) *(volatile LAS u32*)(lds0 + L_QF + 4) = 9;
      }
      pace(0, ((u32)(WO_AT - phase) * tkb) >> 4);
      issue_wo(2 * phase);
      issue_wo(2 * phase + 1);
    }
    bar();
    {
      const f32x2 ml = lds_rf2(a_ml);
      f32x4 ov[4];
#pragma unroll
      for (int i = 0; i < 4; ++i) ov[i] = lds_rf4(a_ov + i * 16384);
      asm volatile("" : "+v"(ov[0]), "+v"(ov[1]), "+v"(ov[2]), "+v"(ov[3]));
      float M = quad_max(ml[0]);
      { auto sw = __builtin_amdgcn_permlane32_swap(__float_as_uint(M), __float_as_uint(M), false, false);
        M = vmax(__uint_as_float(sw[0]), __uint_as_float(sw[1])); }
      const float wt = __builtin_amdgcn_exp2f(ml[0] - M);
      float L = wt * ml[1];
      L += dpp<0xB1>(L); L += dpp<0x4E>(L);
      L = fold32(L, L);
      f32x4 o = dpp<0x00>(wt) * ov[0];
      o += dpp<0x55>(wt) * ov[1];
      o += dpp<0xAA>(wt) * ov[2];
      o += dpp<0xFF>(wt) * ov[3];
      const float o0 = fold32(o[0], o[2]), o1 = fold32(o[1], o[3]);
      u32x4 uo = {__float_as_uint(o0), t1, __float_as_uint(o1), t2};
      st16_sc1(uo, rws, a_pub);
      if (lane < 8) {
        u32x4 um = {__float_as_uint(M), t1, __float_as_uint(L), t2};
        st16_sc1(um, rws, a_pub + 1024);
      }
    }

    s16x4 wf[8];
    wait_vm<2>();
    bar();
#pragma unroll
    for (int c = 0; c < 8; ++c) wf[c] = lds_tr(a_w + c * 4096);
    {
      u32x4 d[5];
      __builtin_amdgcn_s_sleep(D_POLL1);
      poll5(d, rws, a_d, a_e, t1, t2);
      float Mj[4], Lj[4];
#pragma unroll
      for (int j = 0; j < 4; ++j) { Mj[j] = lane_f(d[4][0], j); Lj[j] = lane_f(d[4][2], j); }
      const float M = vmax(vmax(Mj[0], Mj[1]), vmax(Mj[2], Mj[3]));
      float L = 0.f, o0 = 0.f, o1 = 0.f;
#pragma unroll
      for (int j = 0; j < 4; ++j) {
        const float wt = __builtin_amdgcn_exp2f(Mj[j] - M);
        L = __builtin_fmaf(wt, Lj[j], L);
        o0 = __builtin_fmaf(wt, __uint_as_float(d[j][0]), o0);
        o1 = __builtin_fmaf(wt, __uint_as_float(d[j][2]), o1);
      }
      const float inv = __builtin_amdgcn_rcpf(L);
      *(LAS u32*)a_oimg = pk_bf16(o0 * inv, o1 * inv);
    }
    bar();
    f32x4 y = {0.f, 0.f, 0.f, 0.f};
#pragma unroll
    for (int kk = 0; kk < 4; ++kk)
      y = mfma(cat_tr(wf[2 * kk], wf[2 * kk + 1]), __builtin_bit_cast(bf16x8, lds_r128(a_of + kk * 64)), y);
    const float ys0 = n < 8 ? y[0] : y[2], ys1 = n < 8 ? y[1] : y[3];
    const u32 y0 = __builtin_amdgcn_ds_bpermute(y_src, __float_as_uint(ys0)), y1 = __builtin_amdgcn_ds_bpermute(y_src, __float_as_uint(ys1));
    u32x4 uy = {y0, t1, y1, t2};
    st16_sc1(uy, rws, a_y);
  }

  if (w == 0) {
    const int b = xc, c32 = slot & 15;
    const u32 base = WS_Y + (u32)((br2 * 8 + b) * 16 + c32) * (32u * 256u) + lane * 16;
    u32x4 d[8];
    __builtin_amdgcn_s_sleep(D_POLL2);
    poll8(d, rws, base, base + 4096, t1, t2);
    float y0 = 0.f, y1 = 0.f;
#pragma unroll
    for (int i = 0; i < 8; ++i) { y0 += __uint_as_float(d[i][0]); y1 += __uint_as_float(d[i][2]); }
    y0 = xrow_sum(y0); y1 = xrow_sum(y1);
    if (lane < 16) {
      GAS u32* po = (GAS u32*)pout + (b * 1024 + br2 * 512 + c32 * 32) / 2 + lane;
      *po = pk_bf16(y0, y1);
    }
    if (wg == 0 && lane == 0) __hip_atomic_store((GAS u64*)((GAS char*)pws + WS_EPOCH), ep, __ATOMIC_RELAXED, __HIP_MEMORY_SCOPE_AGENT);
  }
  asm volatile(".Lfa_end:");
}

extern "C" void kernel_launch(void* const* d_in, const int* in_sizes, int n_in, void* d_out, int out_size,
                              void* d_ws, size_t ws_size, hipStream_t stream) {
  Params p;
  p.x[0] = d_in[0]; p.k[0] = d_in[1]; p.v[0] = d_in[2]; p.wq[0] = d_in[3]; p.wo[0] = d_in[4];
  p.x[1] = d_in[5]; p.k[1] = d_in[6]; p.v[1] = d_in[7]; p.wq[1] = d_in[8]; p.wo[1] = d_in[9];
  p.out = d_out; p.ws = d_ws;
  fused_attn_kernel<<<256, 512, 0, stream>>>(p);
}
```
